```python
import torch

def _softmax(x, axis=-1):
    return torch.softmax(x, dim=axis)
def _gelu(x):
    return torch.nn.functional.gelu(x, approximate="tanh")
def _silu(x):
    return torch.nn.functional.silu(x)
def _sigmoid(x):
    return torch.sigmoid(x)
def _layernorm(x, axis=-1, eps=1e-5):
    m = x.mean(dim=axis, keepdim=True)
    v = x.var(dim=axis, keepdim=True, unbiased=False)
    return (x - m) / torch.sqrt(v + eps)
def _scatter_add(tgt, idx, src, axis=0):
    return tgt.clone().index_add_(axis, idx, src)
def _segment_sum(src, seg, n):
    out = torch.zeros((n,) + src.shape[1:], dtype=src.dtype)
    return out.index_add_(0, seg, src)
def _pad(x, pw):
    flat = tuple(v for pair in reversed(pw) for v in pair)
    return torch.nn.functional.pad(x, flat)

kernel_name = 'standalone__batchnorm__sampling'

def setup_inputs():

    _w = (lambda a: (lambda t: t.to(torch.bfloat16) if t.is_floating_point() else t)(torch.as_tensor(a)))
    B, S, C = 1024, 4, 512
    K = 3
    return {
        "x": _w((lambda *s: torch.randn(*s, dtype=torch.bfloat16) * 0.05)(B, S, C)),
        "w": _w((lambda *s: torch.randn(*s, dtype=torch.bfloat16) * 0.05)(K, C, C) * (1.0 / (K * C) ** 0.5)),
        "mean": _w((lambda *s: torch.randn(*s, dtype=torch.bfloat16) * 0.05)(C) * 0.1),
        "var": _w(torch.abs((lambda *s: torch.randn(*s, dtype=torch.bfloat16) * 0.05)(C)) + 0.5),
        "g": _w((lambda *s: torch.randn(*s, dtype=torch.bfloat16) * 0.05)(C) + 1.0),
        "b": _w((lambda *s: torch.randn(*s, dtype=torch.bfloat16) * 0.05)(C) * 0.1),
    }

def reference(x, w, mean, var, g, b):
    K = 3
    P = K // 2
    S = x.shape[1]
    xp = _pad(x, ((0, 0), (P, P), (0, 0)))
    y = sum(torch.einsum('bsc,cd->bsd', xp[:, k:k + S, :], w[k]) for k in range(K))
    inv_std = 1.0 / torch.sqrt(var + 1e-5)
    y = (y - mean) * (g * inv_std) + b
    return _silu(y)
```

```cpp
#include <hip/hip_runtime.h>
#include <hip/hip_bf16.h>
#include <stdint.h>

typedef __attribute__((ext_vector_type(8))) __bf16 bf16x8;
typedef __attribute__((ext_vector_type(4))) __bf16 bf16x4;
typedef __attribute__((ext_vector_type(2))) __bf16 bf16x2;
typedef __attribute__((ext_vector_type(4))) short s16x4;
typedef __attribute__((ext_vector_type(16))) float f32x16;
typedef __attribute__((ext_vector_type(4))) float f32x4;
typedef __attribute__((ext_vector_type(2))) float f32x2;
typedef __attribute__((ext_vector_type(4))) unsigned u32x4;
typedef __attribute__((ext_vector_type(2))) unsigned u32x2;

#define LAS __attribute__((address_space(3)))
#define DI __device__ __forceinline__

constexpr int C_ = 512;
constexpr int STAGE_B = 40960;
constexpr int W_OFF = 16384;
constexpr int NSTAGE = 8;
constexpr unsigned SRD3 = 0x00020000u;

DI unsigned rfl(unsigned v) { return __builtin_amdgcn_readfirstlane(v); }

DI void dma16(unsigned m0v, unsigned voff, __amdgpu_buffer_rsrc_t srd, unsigned soff) {
  asm volatile("s_mov_b32 m0, %0\n\ts_nop 0\n\tbuffer_load_dwordx4 %1, %2, %3 offen lds"
               :: "s"(m0v), "v"(voff), "s"(srd), "s"(soff) : "memory");
}

template <int N> DI void wait_vm() { asm volatile("s_waitcnt vmcnt(%0)" :: "n"(N) : "memory"); }
DI void bar() { asm volatile("s_barrier" ::: "memory"); }
DI void bar_lgkm() { asm volatile("s_waitcnt lgkmcnt(0)\n\ts_barrier" ::: "memory"); }

struct Frags { bf16x8 xf[4]; bf16x8 wf[3]; };

DI bf16x8 tr8(const LAS char* p0, int off) {
  s16x4 lo = __builtin_amdgcn_ds_read_tr16_b64_v4i16((LAS s16x4*)(p0 + off));
  s16x4 hi = __builtin_amdgcn_ds_read_tr16_b64_v4i16((LAS s16x4*)(p0 + off + 512));
  typedef __attribute__((ext_vector_type(8))) short s16x8;
  s16x8 v = __builtin_shufflevector(lo, hi, 0, 1, 2, 3, 4, 5, 6, 7);
  return __builtin_bit_cast(bf16x8, v);
}

DI void read_frags(Frags& f, const LAS char* xp, const LAS char* wp) {
#pragma unroll
  for (int t = 0; t < 4; ++t) f.xf[t] = *(const LAS bf16x8*)(xp + t * 4096);
#pragma unroll
  for (int k = 0; k < 3; ++k) f.wf[k] = tr8(wp, k * 8192);
}

DI void mma(f32x16 (&acc)[4], const Frags& f) {
#pragma unroll
  for (int k = 0; k < 3; ++k)
#pragma unroll
    for (int t = 0; t < 4; ++t) {
      int s = t - k + 1;
      if (s >= 0 && s < 4) acc[s] = __builtin_amdgcn_mfma_f32_32x32x16_bf16(f.wf[k], f.xf[t], acc[s], 0, 0, 0);
    }
}

DI float bf_lo(unsigned u) { return __builtin_bit_cast(float, u << 16); }
DI float bf_hi(unsigned u) { return __builtin_bit_cast(float, u & 0xffff0000u); }
DI unsigned pack2(float a, float b) {
  f32x2 v = {a, b};
  return __builtin_bit_cast(unsigned, __builtin_convertvector(v, bf16x2));
}

__global__ __launch_bounds__(512, 2) void conv_bn_silu_kernel(
    const __hip_bfloat16* __restrict__ x, const __hip_bfloat16* __restrict__ w,
    const __hip_bfloat16* __restrict__ mean, const __hip_bfloat16* __restrict__ var,
    const __hip_bfloat16* __restrict__ gam, const __hip_bfloat16* __restrict__ bet,
    __hip_bfloat16* __restrict__ out) {
  __shared__ __attribute__((aligned(1024))) char smem[4 * STAGE_B];
  const unsigned tid = threadIdx.x;
  const unsigned wave = rfl(tid >> 6);
  const unsigned lane = tid & 63;
  const unsigned bid = blockIdx.x;
  const unsigned xcd = bid & 7, idx = bid >> 3;
  const unsigned mb = 8 * (xcd >> 1) + (idx & 7);
  const unsigned nb = 4 * (xcd & 1) + (idx >> 3);
  const unsigned lds0 = (unsigned)(size_t)(LAS char*)smem;

  if (wave >= 4) {
    const unsigned li = wave - 4;
    __amdgpu_buffer_rsrc_t sx = __builtin_amdgcn_make_buffer_rsrc((void*)x, (short)0, 1024 * 4 * C_ * 2, SRD3);
    __amdgpu_buffer_rsrc_t sw = __builtin_amdgcn_make_buffer_rsrc((void*)w, (short)0, 3 * C_ * C_ * 2, SRD3);
    const unsigned r8 = lane >> 3, p = lane & 7;
    const unsigned vx0 = r8 * 4096 + ((p ^ (lane >> 4)) << 4);
    const unsigned vx1 = r8 * 4096 + ((p ^ (4 | (lane >> 4))) << 4);
    const unsigned vw = r8 * 1024 + ((p ^ (((lane >> 4) & 1) << 2)) << 4);
    const unsigned sx0 = (mb * 32 * 4 + li) * 1024;
    const unsigned mx0 = lds0 + li * 4096;
    auto issue = [&](int s) {
      const unsigned slot = (s & 3) * STAGE_B;
#pragma unroll
      for (int j = 0; j < 4; ++j)
        dma16(mx0 + slot + j * 1024, (j & 1) ? vx1 : vx0, sx, sx0 + j * 32768 + s * 128);
#pragma unroll
      for (int j = 0; j < 6; ++j) {
        const unsigned jj = li * 6 + j;
        dma16(lds0 + slot + W_OFF + jj * 1024, vw, sw,
              (jj >> 3) * (C_ * C_ * 2) + (s * 64 + (jj & 7) * 8) * 1024 + nb * 128);
      }
    };
#pragma unroll
    for (int s = 0; s < 4; ++s) issue(s);
    wait_vm<30>(); bar();
    wait_vm<20>(); bar(); issue(4);
    wait_vm<20>(); bar(); issue(5);
    wait_vm<20>(); bar(); issue(6);
    wait_vm<20>(); bar(); issue(7);
    wait_vm<20>(); bar();
    wait_vm<10>(); bar();
    wait_vm<0>(); bar();
    bar();
    return;
  }

  const unsigned kh = wave >> 1, ni = wave & 1;
  const unsigned r = lane & 31, h = lane >> 5;
  const unsigned d0 = nb * 64 + ni * 32 + 8 * h;

  u32x4 pm[2], pv[2], pg[2], pb[2];
#pragma unroll
  for (int i = 0; i < 2; ++i) {
    pm[i] = *(const u32x4*)(mean + d0 + 16 * i);
    pv[i] = *(const u32x4*)(var + d0 + 16 * i);
    pg[i] = *(const u32x4*)(gam + d0 + 16 * i);
    pb[i] = *(const u32x4*)(bet + d0 + 16 * i);
  }
  float sc[16], sh[16];
#pragma unroll
  for (int i = 0; i < 16; ++i) {
    unsigned um = pm[i >> 3][(i >> 1) & 3], uv = pv[i >> 3][(i >> 1) & 3];
    unsigned ug = pg[i >> 3][(i >> 1) & 3], ub = pb[i >> 3][(i >> 1) & 3];
    float m = (i & 1) ? bf_hi(um) : bf_lo(um), v = (i & 1) ? bf_hi(uv) : bf_lo(uv);
    float g = (i & 1) ? bf_hi(ug) : bf_lo(ug), b = (i & 1) ? bf_hi(ub) : bf_lo(ub);
    sc[i] = g * __builtin_amdgcn_rsqf(v + 1e-5f);
    sh[i] = b - m * sc[i];
  }

  const unsigned q = (lane & 15) >> 2, p = lane & 3, G = lane >> 4;
  const unsigned xo0 = r * 128 + (((kh * 4 + h) ^ ((r >> 1) & 7)) << 4);
  const unsigned xo1 = xo0 ^ 32;
  const unsigned wo = W_OFF + (kh * 32 + 8 * h + q) * 128 +
                      (((ni * 4 + 2 * (G & 1) + (p & 1)) ^ ((q >> 1) << 2)) << 4) + 8 * (p >> 1);
  const LAS char* sb = (const LAS char*)smem;

  f32x16 acc[4];
#pragma unroll
  for (int s = 0; s < 4; ++s)
#pragma unroll
    for (int i = 0; i < 16; ++i) acc[s][i] = 0.f;

  Frags fa, fb;
  bar();
  read_frags(fa, sb + xo0, sb + wo);
#pragma unroll
  for (int s = 0; s < NSTAGE; ++s) {
    const int slot = (s & 3) * STAGE_B;
    read_frags(fb, sb + slot + xo1, sb + slot + wo + 2048);
    mma(acc, fa);
    if (s + 1 < NSTAGE) {
      const int nslot = ((s + 1) & 3) * STAGE_B;
      bar_lgkm();
      read_frags(fa, sb + nslot + xo0, sb + nslot + wo);
    }
    mma(acc, fb);
  }

  LAS char* ex = (LAS char*)smem;
  {
    LAS char* dst = ex + wave * 8192 + lane * 16;
#pragma unroll
    for (int a = 0; a < 2; ++a)
#pragma unroll
      for (int i = 0; i < 4; ++i) {
        f32x4 v0 = {acc[a][4 * i], acc[a][4 * i + 1], acc[a][4 * i + 2], acc[a][4 * i + 3]};
        f32x4 v1 = {acc[2 + a][4 * i], acc[2 + a][4 * i + 1], acc[2 + a][4 * i + 2], acc[2 + a][4 * i + 3]};
        f32x4 v = kh ? v0 : v1;
        *(LAS f32x4*)(dst + (a * 4 + i) * 1024) = v;
      }
  }
  bar_lgkm();
  const LAS char* src = ex + (wave ^ 2) * 8192 + lane * 16;
#pragma unroll
  for (int a = 0; a < 2; ++a) {
    float y[16];
#pragma unroll
    for (int i = 0; i < 4; ++i) {
      f32x4 v = *(const LAS f32x4*)(src + (a * 4 + i) * 1024);
#pragma unroll
      for (int j = 0; j < 4; ++j) {
        float own = kh ? acc[2 + a][4 * i + j] : acc[a][4 * i + j];
        y[4 * i + j] = own + v[j];
      }
    }
    unsigned pk[8];
#pragma unroll
    for (int i = 0; i < 16; i += 2) {
      float y0 = y[i] * sc[i] + sh[i], y1 = y[i + 1] * sc[i + 1] + sh[i + 1];
      float e0 = __builtin_amdgcn_exp2f(y0 * -1.4426950408889634f);
      float e1 = __builtin_amdgcn_exp2f(y1 * -1.4426950408889634f);
      float o0 = y0 * __builtin_amdgcn_rcpf(1.f + e0), o1 = y1 * __builtin_amdgcn_rcpf(1.f + e1);
      pk[i >> 1] = pack2(o0, o1);
    }
    const unsigned s = kh * 2 + a;
    __hip_bfloat16* op = out + ((mb * 32 + r) * 4 + s) * C_ + d0;
    u32x4 o0 = {pk[0], pk[1], pk[2], pk[3]}, o1 = {pk[4], pk[5], pk[6], pk[7]};
    *(u32x4*)op = o0;
    *(u32x4*)(op + 16) = o1;
  }
}

extern "C" void kernel_launch(void* const* d_in, const int* in_sizes, int n_in, void* d_out, int out_size,
                              void* d_ws, size_t ws_size, hipStream_t stream) {
  conv_bn_silu_kernel<<<256, 512, 0, stream>>>(
      (const __hip_bfloat16*)d_in[0], (const __hip_bfloat16*)d_in[1], (const __hip_bfloat16*)d_in[2],
      (const __hip_bfloat16*)d_in[3], (const __hip_bfloat16*)d_in[4], (const __hip_bfloat16*)d_in[5],
      (__hip_bfloat16*)d_out);
}
```

```cpp
#include <hip/hip_runtime.h>
#include <hip/hip_bf16.h>
#include <stdint.h>
#define STAMPS
#include <stdio.h>
#define NSTAMP 16
#define STAMP_ARG , unsigned long long* __restrict__ stamps
#define STAMP_PASS , (unsigned long long*)d_ws
#define STAMP_DECL unsigned long long st_[NSTAMP]; _Pragma("unroll") for (int i_ = 0; i_ < NSTAMP; ++i_) st_[i_] = 0; \
  st_[0] = __builtin_amdgcn_s_memrealtime(); st_[1] = __builtin_amdgcn_s_memtime();
#define STAMP(i) do { asm volatile("" ::: "memory"); st_[i] = __builtin_amdgcn_s_memtime(); asm volatile("" ::: "memory"); } while (0)
#define STAMP_END do { asm volatile("s_waitcnt vmcnt(0)" ::: "memory"); st_[NSTAMP - 2] = __builtin_amdgcn_s_memtime(); st_[NSTAMP - 1] = __builtin_amdgcn_s_memrealtime(); \
  if ((threadIdx.x & 63) == 0) { _Pragma("unroll") for (int i_ = 0; i_ < NSTAMP; ++i_) stamps[(blockIdx.x * 8 + (threadIdx.x >> 6)) * NSTAMP + i_] = st_[i_]; } } while (0)

typedef __attribute__((ext_vector_type(8))) __bf16 bf16x8;
typedef __attribute__((ext_vector_type(4))) __bf16 bf16x4;
typedef __attribute__((ext_vector_type(2))) __bf16 bf16x2;
typedef __attribute__((ext_vector_type(4))) short s16x4;
typedef __attribute__((ext_vector_type(16))) float f32x16;
typedef __attribute__((ext_vector_type(4))) float f32x4;
typedef __attribute__((ext_vector_type(2))) float f32x2;
typedef __attribute__((ext_vector_type(4))) unsigned u32x4;
typedef __attribute__((ext_vector_type(2))) unsigned u32x2;

#define LAS __attribute__((address_space(3)))
#define DI __device__ __forceinline__

constexpr int C_ = 512;
constexpr int STAGE_B = 40960;
constexpr int W_OFF = 16384;
constexpr int NSTAGE = 8;
constexpr unsigned SRD3 = 0x00020000u;

DI unsigned rfl(unsigned v) { return __builtin_amdgcn_readfirstlane(v); }

DI void dma16(unsigned m0v, unsigned voff, __amdgpu_buffer_rsrc_t srd, unsigned soff) {
  asm volatile("s_mov_b32 m0, %0\n\ts_nop 0\n\tbuffer_load_dwordx4 %1, %2, %3 offen lds"
               :: "s"(m0v), "v"(voff), "s"(srd), "s"(soff) : "memory");
}

template <int N> DI void wait_vm() { asm volatile("s_waitcnt vmcnt(%0)" :: "n"(N) : "memory"); }
DI void bar() { asm volatile("s_barrier" ::: "memory"); }
DI void bar_lgkm() { asm volatile("s_waitcnt lgkmcnt(0)\n\ts_barrier" ::: "memory"); }

struct Frags { bf16x8 xf[4]; bf16x8 wf[3]; };

DI bf16x8 tr8(const LAS char* p0, int off) {
  s16x4 lo = __builtin_amdgcn_ds_read_tr16_b64_v4i16((LAS s16x4*)(p0 + off));
  s16x4 hi = __builtin_amdgcn_ds_read_tr16_b64_v4i16((LAS s16x4*)(p0 + off + 512));
  typedef __attribute__((ext_vector_type(8))) short s16x8;
  s16x8 v = __builtin_shufflevector(lo, hi, 0, 1, 2, 3, 4, 5, 6, 7);
  return __builtin_bit_cast(bf16x8, v);
}

DI void read_frags(Frags& f, const LAS char* xp, const LAS char* wp) {
#pragma unroll
  for (int t = 0; t < 4; ++t) f.xf[t] = *(const LAS bf16x8*)(xp + t * 4096);
#pragma unroll
  for (int k = 0; k < 3; ++k) f.wf[k] = tr8(wp, k * 8192);
}

DI void mma(f32x16 (&acc)[4], const Frags& f) {
#pragma unroll
  for (int k = 0; k < 3; ++k)
#pragma unroll
    for (int t = 0; t < 4; ++t) {
      int s = t - k + 1;
      if (s >= 0 && s < 4) acc[s] = __builtin_amdgcn_mfma_f32_32x32x16_bf16(f.wf[k], f.xf[t], acc[s], 0, 0, 0);
    }
}

DI float bf_lo(unsigned u) { return __builtin_bit_cast(float, u << 16); }
DI float bf_hi(unsigned u) { return __builtin_bit_cast(float, u & 0xffff0000u); }
DI unsigned pack2(float a, float b) {
  f32x2 v = {a, b};
  return __builtin_bit_cast(unsigned, __builtin_convertvector(v, bf16x2));
}

__global__ __launch_bounds__(512, 2) void conv_bn_silu_kernel(
    const __hip_bfloat16* __restrict__ x, const __hip_bfloat16* __restrict__ w,
    const __hip_bfloat16* __restrict__ mean, const __hip_bfloat16* __restrict__ var,
    const __hip_bfloat16* __restrict__ gam, const __hip_bfloat16* __restrict__ bet,
    __hip_bfloat16* __restrict__ out STAMP_ARG) {
  STAMP_DECL
  __shared__ __attribute__((aligned(1024))) char smem[4 * STAGE_B];
  const unsigned tid = threadIdx.x;
  const unsigned wave = rfl(tid >> 6);
  const unsigned lane = tid & 63;
  const unsigned bid = blockIdx.x;
  const unsigned xcd = bid & 7, idx = bid >> 3;
  const unsigned mb = 8 * (xcd >> 1) + (idx & 7);
  const unsigned nb = 4 * (xcd & 1) + (idx >> 3);
  const unsigned lds0 = (unsigned)(size_t)(LAS char*)smem;

  if (wave >= 4) {
    const unsigned li = wave - 4;
    __amdgpu_buffer_rsrc_t sx = __builtin_amdgcn_make_buffer_rsrc((void*)x, (short)0, 1024 * 4 * C_ * 2, SRD3);
    __amdgpu_buffer_rsrc_t sw = __builtin_amdgcn_make_buffer_rsrc((void*)w, (short)0, 3 * C_ * C_ * 2, SRD3);
    const unsigned r8 = lane >> 3, p = lane & 7;
    const unsigned vx0 = r8 * 4096 + ((p ^ (lane >> 4)) << 4);
    const unsigned vx1 = r8 * 4096 + ((p ^ (4 | (lane >> 4))) << 4);
    const unsigned vw = r8 * 1024 + ((p ^ (((lane >> 4) & 1) << 2)) << 4);
    const unsigned sx0 = (mb * 32 * 4 + li) * 1024;
    const unsigned mx0 = lds0 + li * 4096;
    auto issue = [&](int s) {
      const unsigned slot = (s & 3) * STAGE_B;
#pragma unroll
      for (int j = 0; j < 4; ++j)
        dma16(mx0 + slot + j * 1024, (j & 1) ? vx1 : vx0, sx, sx0 + j * 32768 + s * 128);
#pragma unroll
      for (int j = 0; j < 6; ++j) {
        const unsigned jj = li * 6 + j;
        dma16(lds0 + slot + W_OFF + jj * 1024, vw, sw,
              (jj >> 3) * (C_ * C_ * 2) + (s * 64 + (jj & 7) * 8) * 1024 + nb * 128);
      }
    };
#pragma unroll
    for (int s = 0; s < 4; ++s) issue(s);
    STAMP(2);
    wait_vm<30>(); STAMP(3); bar();
    wait_vm<20>(); STAMP(4); bar(); issue(4);
    wait_vm<20>(); STAMP(5); bar(); issue(5);
    wait_vm<20>(); STAMP(6); bar(); issue(6);
    wait_vm<20>(); STAMP(7); bar(); issue(7);
    wait_vm<20>(); STAMP(8); bar();
    wait_vm<10>(); STAMP(9); bar();
    wait_vm<0>(); STAMP(10); bar();
    bar();
    STAMP_END;
    return;
  }

  const unsigned kh = wave >> 1, ni = wave & 1;
  const unsigned r = lane & 31, h = lane >> 5;
  const unsigned d0 = nb * 64 + ni * 32 + 8 * h;

  u32x4 pm[2], pv[2], pg[2], pb[2];
#pragma unroll
  for (int i = 0; i < 2; ++i) {
    pm[i] = *(const u32x4*)(mean + d0 + 16 * i);
    pv[i] = *(const u32x4*)(var + d0 + 16 * i);
    pg[i] = *(const u32x4*)(gam + d0 + 16 * i);
    pb[i] = *(const u32x4*)(bet + d0 + 16 * i);
  }
  float sc[16], sh[16];
#pragma unroll
  for (int i = 0; i < 16; ++i) {
    unsigned um = pm[i >> 3][(i >> 1) & 3], uv = pv[i >> 3][(i >> 1) & 3];
    unsigned ug = pg[i >> 3][(i >> 1) & 3], ub = pb[i >> 3][(i >> 1) & 3];
    float m = (i & 1) ? bf_hi(um) : bf_lo(um), v = (i & 1) ? bf_hi(uv) : bf_lo(uv);
    float g = (i & 1) ? bf_hi(ug) : bf_lo(ug), b = (i & 1) ? bf_hi(ub) : bf_lo(ub);
    sc[i] = g * __builtin_amdgcn_rsqf(v + 1e-5f);
    sh[i] = b - m * sc[i];
  }

  const unsigned q = (lane & 15) >> 2, p = lane & 3, G = lane >> 4;
  const unsigned xo0 = r * 128 + (((kh * 4 + h) ^ ((r >> 1) & 7)) << 4);
  const unsigned xo1 = xo0 ^ 32;
  const unsigned wo = W_OFF + (kh * 32 + 8 * h + q) * 128 +
                      (((ni * 4 + 2 * (G & 1) + (p & 1)) ^ ((q >> 1) << 2)) << 4) + 8 * (p >> 1);
  const LAS char* sb = (const LAS char*)smem;

  f32x16 acc[4];
#pragma unroll
  for (int s = 0; s < 4; ++s)
#pragma unroll
    for (int i = 0; i < 16; ++i) acc[s][i] = 0.f;

  Frags fa, fb;
  STAMP(2);
  bar();
  STAMP(3);
  read_frags(fa, sb + xo0, sb + wo);
#pragma unroll
  for (int s = 0; s < NSTAGE; ++s) {
    const int slot = (s & 3) * STAGE_B;
    read_frags(fb, sb + slot + xo1, sb + slot + wo + 2048);
    mma(acc, fa);
    if (s + 1 < NSTAGE) {
      const int nslot = ((s + 1) & 3) * STAGE_B;
      bar_lgkm();
      STAMP(4 + s);
      read_frags(fa, sb + nslot + xo0, sb + nslot + wo);
    }
    mma(acc, fb);
  }

  LAS char* ex = (LAS char*)smem;
  {
    LAS char* dst = ex + wave * 8192 + lane * 16;
#pragma unroll
    for (int a = 0; a < 2; ++a)
#pragma unroll
      for (int i = 0; i < 4; ++i) {
        f32x4 v0 = {acc[a][4 * i], acc[a][4 * i + 1], acc[a][4 * i + 2], acc[a][4 * i + 3]};
        f32x4 v1 = {acc[2 + a][4 * i], acc[2 + a][4 * i + 1], acc[2 + a][4 * i + 2], acc[2 + a][4 * i + 3]};
        f32x4 v = kh ? v0 : v1;
        *(LAS f32x4*)(dst + (a * 4 + i) * 1024) = v;
      }
  }
  STAMP(11);
  bar_lgkm();
  STAMP(12);
  const LAS char* src = ex + (wave ^ 2) * 8192 + lane * 16;
#pragma unroll
  for (int a = 0; a < 2; ++a) {
    float y[16];
#pragma unroll
    for (int i = 0; i < 4; ++i) {
      f32x4 v = *(const LAS f32x4*)(src + (a * 4 + i) * 1024);
#pragma unroll
      for (int j = 0; j < 4; ++j) {
        float own = kh ? acc[2 + a][4 * i + j] : acc[a][4 * i + j];
        y[4 * i + j] = own + v[j];
      }
    }
    unsigned pk[8];
#pragma unroll
    for (int i = 0; i < 16; i += 2) {
      float y0 = y[i] * sc[i] + sh[i], y1 = y[i + 1] * sc[i + 1] + sh[i + 1];
      float e0 = __builtin_amdgcn_exp2f(y0 * -1.4426950408889634f);
      float e1 = __builtin_amdgcn_exp2f(y1 * -1.4426950408889634f);
      float o0 = y0 * __builtin_amdgcn_rcpf(1.f + e0), o1 = y1 * __builtin_amdgcn_rcpf(1.f + e1);
      pk[i >> 1] = pack2(o0, o1);
    }
    const unsigned s = kh * 2 + a;
    __hip_bfloat16* op = out + ((mb * 32 + r) * 4 + s) * C_ + d0;
    u32x4 o0 = {pk[0], pk[1], pk[2], pk[3]}, o1 = {pk[4], pk[5], pk[6], pk[7]};
    *(u32x4*)op = o0;
    *(u32x4*)(op + 16) = o1;
  }
  STAMP(13);
  STAMP_END;
}

__global__ void report_kernel(const unsigned long long* __restrict__ s) {
  unsigned long long rt0 = ~0ull, rt1 = 0, rs1 = 0;
  for (int b = threadIdx.x; b < 256; b += 64)
    for (int w = 0; w < 8; ++w) {
      const unsigned long long* p = s + (b * 8 + w) * NSTAMP;
      rt0 = p[0] < rt0 ? p[0] : rt0; rs1 = p[0] > rs1 ? p[0] : rs1; rt1 = p[NSTAMP - 1] > rt1 ? p[NSTAMP - 1] : rt1;
    }
  for (int o = 32; o; o >>= 1) {
    unsigned long long a = __shfl_xor(rt0, o), b = __shfl_xor(rt1, o), c = __shfl_xor(rs1, o);
    rt0 = a < rt0 ? a : rt0; rt1 = b > rt1 ? b : rt1; rs1 = c > rs1 ? c : rs1;
  }
  if (threadIdx.x == 0) {
    const unsigned long long* p = s + (100 * 8 + 0) * NSTAMP;
    const unsigned long long* q = s + (100 * 8 + 4) * NSTAMP;
    printf("T %llu span %d skew %d | w0 rt %d: %d %d %d %d %d %d %d %d | w4: %d %d %d\n", rt0 / 100, (int)(rt1 - rt0), (int)(rs1 - rt0),
      (int)(p[15] - p[0]), (int)(p[2] - p[1]), (int)(p[3] - p[1]), (int)(p[4] - p[1]), (int)(p[10] - p[1]), (int)(p[11] - p[1]), (int)(p[12] - p[1]), (int)(p[13] - p[1]), (int)(p[14] - p[1]),
      (int)(q[2] - q[1]), (int)(q[4] - q[1]), (int)(q[10] - q[1]));
  }
}

extern "C" void kernel_launch(void* const* d_in, const int* in_sizes, int n_in, void* d_out, int out_size,
                              void* d_ws, size_t ws_size, hipStream_t stream) {
  conv_bn_silu_kernel<<<256, 512, 0, stream>>>(
      (const __hip_bfloat16*)d_in[0], (const __hip_bfloat16*)d_in[1], (const __hip_bfloat16*)d_in[2],
      (const __hip_bfloat16*)d_in[3], (const __hip_bfloat16*)d_in[4], (const __hip_bfloat16*)d_in[5],
      (__hip_bfloat16*)d_out STAMP_PASS);
  report_kernel<<<1, 64, 0, stream>>>((const unsigned long long*)d_ws);
}
```

```cpp
#include <hip/hip_runtime.h>
#include <hip/hip_bf16.h>
#include <stdint.h>
#define STAMPS
#define NSTAMP 16
#define STAMP_ARG , unsigned long long* __restrict__ stamps
#define STAMP_PASS , (unsigned long long*)d_ws
#define STAMP_DECL unsigned long long st_[NSTAMP]; _Pragma("unroll") for (int i_ = 0; i_ < NSTAMP; ++i_) st_[i_] = 0; \
  st_[0] = __builtin_amdgcn_s_memrealtime(); st_[1] = __builtin_amdgcn_s_memtime();
#define STAMP(i) do { asm volatile("" ::: "memory"); st_[i] = __builtin_amdgcn_s_memtime(); asm volatile("" ::: "memory"); } while (0)
#define STAMP_END do { asm volatile("s_waitcnt vmcnt(0)" ::: "memory"); st_[NSTAMP - 2] = __builtin_amdgcn_s_memtime(); st_[NSTAMP - 1] = __builtin_amdgcn_s_memrealtime(); \
  if ((threadIdx.x & 63) == 0) { _Pragma("unroll") for (int i_ = 0; i_ < NSTAMP; ++i_) stamps[(blockIdx.x * 8 + (threadIdx.x >> 6)) * NSTAMP + i_] = st_[i_]; } } while (0)

typedef __attribute__((ext_vector_type(8))) __bf16 bf16x8;
typedef __attribute__((ext_vector_type(4))) __bf16 bf16x4;
typedef __attribute__((ext_vector_type(2))) __bf16 bf16x2;
typedef __attribute__((ext_vector_type(4))) short s16x4;
typedef __attribute__((ext_vector_type(16))) float f32x16;
typedef __attribute__((ext_vector_type(4))) float f32x4;
typedef __attribute__((ext_vector_type(2))) float f32x2;
typedef __attribute__((ext_vector_type(4))) unsigned u32x4;
typedef __attribute__((ext_vector_type(2))) unsigned u32x2;

#define LAS __attribute__((address_space(3)))
#define DI __device__ __forceinline__

constexpr int C_ = 512;
constexpr int STAGE_B = 40960;
constexpr int W_OFF = 16384;
constexpr int NSTAGE = 8;
constexpr unsigned SRD3 = 0x00020000u;

DI unsigned rfl(unsigned v) { return __builtin_amdgcn_readfirstlane(v); }

DI void dma16(unsigned m0v, unsigned voff, __amdgpu_buffer_rsrc_t srd, unsigned soff) {
  asm volatile("s_mov_b32 m0, %0\n\ts_nop 0\n\tbuffer_load_dwordx4 %1, %2, %3 offen lds"
               :: "s"(m0v), "v"(voff), "s"(srd), "s"(soff) : "memory");
}

template <int N> DI void wait_vm() { asm volatile("s_waitcnt vmcnt(%0)" :: "n"(N) : "memory"); }
DI void bar() { asm volatile("s_barrier" ::: "memory"); }
DI void bar_lgkm() { asm volatile("s_waitcnt lgkmcnt(0)\n\ts_barrier" ::: "memory"); }

struct Frags { bf16x8 xf[4]; bf16x8 wf[3]; };

DI bf16x8 tr8(const LAS char* p0, int off) {
  s16x4 lo = __builtin_amdgcn_ds_read_tr16_b64_v4i16((LAS s16x4*)(p0 + off));
  s16x4 hi = __builtin_amdgcn_ds_read_tr16_b64_v4i16((LAS s16x4*)(p0 + off + 512));
  typedef __attribute__((ext_vector_type(8))) short s16x8;
  s16x8 v = __builtin_shufflevector(lo, hi, 0, 1, 2, 3, 4, 5, 6, 7);
  return __builtin_bit_cast(bf16x8, v);
}

DI void read_frags(Frags& f, const LAS char* xp, const LAS char* wp) {
#pragma unroll
  for (int t = 0; t < 4; ++t) f.xf[t] = *(const LAS bf16x8*)(xp + t * 4096);
#pragma unroll
  for (int k = 0; k < 3; ++k) f.wf[k] = tr8(wp, k * 8192);
}

DI void mma(f32x16 (&acc)[4], const Frags& f) {
#pragma unroll
  for (int k = 0; k < 3; ++k)
#pragma unroll
    for (int t = 0; t < 4; ++t) {
      int s = t - k + 1;
      if (s >= 0 && s < 4) acc[s] = __builtin_amdgcn_mfma_f32_32x32x16_bf16(f.wf[k], f.xf[t], acc[s], 0, 0, 0);
    }
}

DI float bf_lo(unsigned u) { return __builtin_bit_cast(float, u << 16); }
DI float bf_hi(unsigned u) { return __builtin_bit_cast(float, u & 0xffff0000u); }
DI unsigned pack2(float a, float b) {
  f32x2 v = {a, b};
  return __builtin_bit_cast(unsigned, __builtin_convertvector(v, bf16x2));
}

__global__ __launch_bounds__(512, 2) void conv_bn_silu_kernel(
    const __hip_bfloat16* __restrict__ x, const __hip_bfloat16* __restrict__ w,
    const __hip_bfloat16* __restrict__ mean, const __hip_bfloat16* __restrict__ var,
    const __hip_bfloat16* __restrict__ gam, const __hip_bfloat16* __restrict__ bet,
    __hip_bfloat16* __restrict__ out STAMP_ARG) {
  STAMP_DECL
  __shared__ __attribute__((aligned(1024))) char smem[4 * STAGE_B];
  const unsigned tid = threadIdx.x;
  const unsigned wave = rfl(tid >> 6);
  const unsigned lane = tid & 63;
  const unsigned bid = blockIdx.x;
  const unsigned xcd = bid & 7, idx = bid >> 3;
  const unsigned mb = 8 * (xcd >> 1) + (idx & 7);
  const unsigned nb = 4 * (xcd & 1) + (idx >> 3);
  const unsigned lds0 = (unsigned)(size_t)(LAS char*)smem;

  if (wave >= 4) {
    const unsigned li = wave - 4;
    __amdgpu_buffer_rsrc_t sx = __builtin_amdgcn_make_buffer_rsrc((void*)x, (short)0, 1024 * 4 * C_ * 2, SRD3);
    __amdgpu_buffer_rsrc_t sw = __builtin_amdgcn_make_buffer_rsrc((void*)w, (short)0, 3 * C_ * C_ * 2, SRD3);
    const unsigned r8 = lane >> 3, p = lane & 7;
    const unsigned vx0 = r8 * 4096 + ((p ^ (lane >> 4)) << 4);
    const unsigned vx1 = r8 * 4096 + ((p ^ (4 | (lane >> 4))) << 4);
    const unsigned vw = r8 * 1024 + ((p ^ (((lane >> 4) & 1) << 2)) << 4);
    const unsigned sx0 = (mb * 32 * 4 + li) * 1024;
    const unsigned mx0 = lds0 + li * 4096;
    auto issue = [&](int s) {
      const unsigned slot = (s & 3) * STAGE_B;
#pragma unroll
      for (int j = 0; j < 4; ++j)
        dma16(mx0 + slot + j * 1024, (j & 1) ? vx1 : vx0, sx, sx0 + j * 32768 + s * 128);
#pragma unroll
      for (int j = 0; j < 6; ++j) {
        const unsigned jj = li * 6 + j;
        dma16(lds0 + slot + W_OFF + jj * 1024, vw, sw,
              (jj >> 3) * (C_ * C_ * 2) + (s * 64 + (jj & 7) * 8) * 1024 + nb * 128);
      }
    };
#pragma unroll
    for (int s = 0; s < 4; ++s) issue(s);
    STAMP(2);
    wait_vm<30>(); STAMP(3); bar();
    wait_vm<20>(); STAMP(4); bar(); issue(4);
    wait_vm<20>(); STAMP(5); bar(); issue(5);
    wait_vm<20>(); STAMP(6); bar(); issue(6);
    wait_vm<20>(); STAMP(7); bar(); issue(7);
    wait_vm<20>(); STAMP(8); bar();
    wait_vm<10>(); STAMP(9); bar();
    wait_vm<0>(); STAMP(10); bar();
    bar();
    STAMP_END;
    return;
  }

  const unsigned kh = wave >> 1, ni = wave & 1;
  const unsigned r = lane & 31, h = lane >> 5;
  const unsigned d0 = nb * 64 + ni * 32 + 8 * h;

  u32x4 pm[2], pv[2], pg[2], pb[2];
#pragma unroll
  for (int i = 0; i < 2; ++i) {
    pm[i] = *(const u32x4*)(mean + d0 + 16 * i);
    pv[i] = *(const u32x4*)(var + d0 + 16 * i);
    pg[i] = *(const u32x4*)(gam + d0 + 16 * i);
    pb[i] = *(const u32x4*)(bet + d0 + 16 * i);
  }
  float sc[16], sh[16];
#pragma unroll
  for (int i = 0; i < 16; ++i) {
    unsigned um = pm[i >> 3][(i >> 1) & 3], uv = pv[i >> 3][(i >> 1) & 3];
    unsigned ug = pg[i >> 3][(i >> 1) & 3], ub = pb[i >> 3][(i >> 1) & 3];
    float m = (i & 1) ? bf_hi(um) : bf_lo(um), v = (i & 1) ? bf_hi(uv) : bf_lo(uv);
    float g = (i & 1) ? bf_hi(ug) : bf_lo(ug), b = (i & 1) ? bf_hi(ub) : bf_lo(ub);
    sc[i] = g * __builtin_amdgcn_rsqf(v + 1e-5f);
    sh[i] = b - m * sc[i];
  }

  const unsigned q = (lane & 15) >> 2, p = lane & 3, G = lane >> 4;
  const unsigned xo0 = r * 128 + (((kh * 4 + h) ^ ((r >> 1) & 7)) << 4);
  const unsigned xo1 = xo0 ^ 32;
  const unsigned wo = W_OFF + (kh * 32 + 8 * h + q) * 128 +
                      (((ni * 4 + 2 * (G & 1) + (p & 1)) ^ ((q >> 1) << 2)) << 4) + 8 * (p >> 1);
  const LAS char* sb = (const LAS char*)smem;

  f32x16 acc[4];
#pragma unroll
  for (int s = 0; s < 4; ++s)
#pragma unroll
    for (int i = 0; i < 16; ++i) acc[s][i] = 0.f;

  Frags fa, fb;
  STAMP(2);
  bar();
  STAMP(3);
  read_frags(fa, sb + xo0, sb + wo);
#pragma unroll
  for (int s = 0; s < NSTAGE; ++s) {
    const int slot = (s & 3) * STAGE_B;
    read_frags(fb, sb + slot + xo1, sb + slot + wo + 2048);
    mma(acc, fa);
    if (s + 1 < NSTAGE) {
      const int nslot = ((s + 1) & 3) * STAGE_B;
      bar_lgkm();
      STAMP(4 + s);
      read_frags(fa, sb + nslot + xo0, sb + nslot + wo);
    }
    mma(acc, fb);
  }

  LAS char* ex = (LAS char*)smem;
  {
    LAS char* dst = ex + wave * 8192 + lane * 16;
#pragma unroll
    for (int a = 0; a < 2; ++a)
#pragma unroll
      for (int i = 0; i < 4; ++i) {
        f32x4 v0 = {acc[a][4 * i], acc[a][4 * i + 1], acc[a][4 * i + 2], acc[a][4 * i + 3]};
        f32x4 v1 = {acc[2 + a][4 * i], acc[2 + a][4 * i + 1], acc[2 + a][4 * i + 2], acc[2 + a][4 * i + 3]};
        f32x4 v = kh ? v0 : v1;
        *(LAS f32x4*)(dst + (a * 4 + i) * 1024) = v;
      }
  }
  STAMP(11);
  bar_lgkm();
  STAMP(12);
  const LAS char* src = ex + (wave ^ 2) * 8192 + lane * 16;
#pragma unroll
  for (int a = 0; a < 2; ++a) {
    float y[16];
#pragma unroll
    for (int i = 0; i < 4; ++i) {
      f32x4 v = *(const LAS f32x4*)(src + (a * 4 + i) * 1024);
#pragma unroll
      for (int j = 0; j < 4; ++j) {
        float own = kh ? acc[2 + a][4 * i + j] : acc[a][4 * i + j];
        y[4 * i + j] = own + v[j];
      }
    }
    unsigned pk[8];
#pragma unroll
    for (int i = 0; i < 16; i += 2) {
      float y0 = y[i] * sc[i] + sh[i], y1 = y[i + 1] * sc[i + 1] + sh[i + 1];
      float e0 = __builtin_amdgcn_exp2f(y0 * -1.4426950408889634f);
      float e1 = __builtin_amdgcn_exp2f(y1 * -1.4426950408889634f);
      float o0 = y0 * __builtin_amdgcn_rcpf(1.f + e0), o1 = y1 * __builtin_amdgcn_rcpf(1.f + e1);
      pk[i >> 1] = pack2(o0, o1);
    }
    const unsigned s = kh * 2 + a;
    __hip_bfloat16* op = out + ((mb * 32 + r) * 4 + s) * C_ + d0;
    u32x4 o0 = {pk[0], pk[1], pk[2], pk[3]}, o1 = {pk[4], pk[5], pk[6], pk[7]};
    *(u32x4*)op = o0;
    *(u32x4*)(op + 16) = o1;
  }
  STAMP(13);
  STAMP_END;
}

__global__ void report_kernel(const unsigned long long* __restrict__ s) {
  unsigned long long rt0 = ~0ull, rt1 = 0, rs1 = 0;
  long long sum = 0;
  for (int b = threadIdx.x; b < 256; b += 64) {
    for (int w = 0; w < 8; ++w) {
      const unsigned long long* p = s + (b * 8 + w) * NSTAMP;
      rt0 = p[0] < rt0 ? p[0] : rt0; rs1 = p[0] > rs1 ? p[0] : rs1; rt1 = p[NSTAMP - 1] > rt1 ? p[NSTAMP - 1] : rt1;
    }
    const unsigned long long* p = s + (b * 8 + 0) * NSTAMP;
    const unsigned long long* q = s + (b * 8 + 4) * NSTAMP;
    sum += (long long)(q[2]-q[1]);
  }
  for (int o = 32; o; o >>= 1) {
    unsigned long long a = __shfl_xor(rt0, o), b = __shfl_xor(rt1, o), c = __shfl_xor(rs1, o);
    rt0 = a < rt0 ? a : rt0; rt1 = b > rt1 ? b : rt1; rs1 = c > rs1 ? c : rs1;
    sum += __shfl_xor(sum, o);
  }
  long long avg = sum / 256;
  long long span = (long long)(rt1 - rt0), skew = (long long)(rs1 - rt0);
  long long v = (avg) * 1;
  if (v < 0) v = 0;
  if (v > 100000) v = 100000;
  unsigned long long t0 = __builtin_amdgcn_s_memrealtime();
  while ((long long)(__builtin_amdgcn_s_memrealtime() - t0) < v) __builtin_amdgcn_s_sleep(1);
}

extern "C" void kernel_launch(void* const* d_in, const int* in_sizes, int n_in, void* d_out, int out_size,
                              void* d_ws, size_t ws_size, hipStream_t stream) {
  conv_bn_silu_kernel<<<256, 512, 0, stream>>>(
      (const __hip_bfloat16*)d_in[0], (const __hip_bfloat16*)d_in[1], (const __hip_bfloat16*)d_in[2],
      (const __hip_bfloat16*)d_in[3], (const __hip_bfloat16*)d_in[4], (const __hip_bfloat16*)d_in[5],
      (__hip_bfloat16*)d_out STAMP_PASS);
  report_kernel<<<1, 64, 0, stream>>>((const unsigned long long*)d_ws);
}
```

```cpp
#include <hip/hip_runtime.h>
#include <hip/hip_bf16.h>
#include <stdint.h>
#define STAMPS
#define NSTAMP 16
#define STAMP_ARG , unsigned long long* __restrict__ stamps
#define STAMP_PASS , (unsigned long long*)d_ws
#define STAMP_DECL unsigned long long st_[NSTAMP]; _Pragma("unroll") for (int i_ = 0; i_ < NSTAMP; ++i_) st_[i_] = 0; \
  st_[0] = __builtin_amdgcn_s_memrealtime(); st_[1] = __builtin_amdgcn_s_memtime();
#define STAMP(i) do { asm volatile("" ::: "memory"); st_[i] = __builtin_amdgcn_s_memtime(); asm volatile("" ::: "memory"); } while (0)
#define STAMP_END do { asm volatile("s_waitcnt vmcnt(0)" ::: "memory"); st_[NSTAMP - 2] = __builtin_amdgcn_s_memtime(); st_[NSTAMP - 1] = __builtin_amdgcn_s_memrealtime(); \
  if ((threadIdx.x & 63) == 0) { _Pragma("unroll") for (int i_ = 0; i_ < NSTAMP; ++i_) stamps[(blockIdx.x * 8 + (threadIdx.x >> 6)) * NSTAMP + i_] = st_[i_]; } } while (0)

typedef __attribute__((ext_vector_type(8))) __bf16 bf16x8;
typedef __attribute__((ext_vector_type(4))) __bf16 bf16x4;
typedef __attribute__((ext_vector_type(2))) __bf16 bf16x2;
typedef __attribute__((ext_vector_type(4))) short s16x4;
typedef __attribute__((ext_vector_type(16))) float f32x16;
typedef __attribute__((ext_vector_type(4))) float f32x4;
typedef __attribute__((ext_vector_type(2))) float f32x2;
typedef __attribute__((ext_vector_type(4))) unsigned u32x4;
typedef __attribute__((ext_vector_type(2))) unsigned u32x2;

#define LAS __attribute__((address_space(3)))
#define DI __device__ __forceinline__

constexpr int C_ = 512;
constexpr int STAGE_B = 40960;
constexpr int W_OFF = 16384;
constexpr int NSTAGE = 8;
constexpr unsigned SRD3 = 0x00020000u;

DI unsigned rfl(unsigned v) { return __builtin_amdgcn_readfirstlane(v); }

DI void dma16(unsigned m0v, unsigned voff, __amdgpu_buffer_rsrc_t srd, unsigned soff) {
  asm volatile("s_mov_b32 m0, %0\n\ts_nop 0\n\tbuffer_load_dwordx4 %1, %2, %3 offen lds"
               :: "s"(m0v), "v"(voff), "s"(srd), "s"(soff) : "memory");
}

template <int N> DI void wait_vm() { asm volatile("s_waitcnt vmcnt(%0)" :: "n"(N) : "memory"); }
DI void bar() { asm volatile("s_barrier" ::: "memory"); }
DI void bar_lgkm() { asm volatile("s_waitcnt lgkmcnt(0)\n\ts_barrier" ::: "memory"); }

struct Frags { bf16x8 xf[4]; bf16x8 wf[3]; };

DI bf16x8 tr8(const LAS char* p0, int off) {
  s16x4 lo = __builtin_amdgcn_ds_read_tr16_b64_v4i16((LAS s16x4*)(p0 + off));
  s16x4 hi = __builtin_amdgcn_ds_read_tr16_b64_v4i16((LAS s16x4*)(p0 + off + 512));
  typedef __attribute__((ext_vector_type(8))) short s16x8;
  s16x8 v = __builtin_shufflevector(lo, hi, 0, 1, 2, 3, 4, 5, 6, 7);
  return __builtin_bit_cast(bf16x8, v);
}

DI void read_frags(Frags& f, const LAS char* xp, const LAS char* wp) {
#pragma unroll
  for (int t = 0; t < 4; ++t) f.xf[t] = *(const LAS bf16x8*)(xp + t * 4096);
#pragma unroll
  for (int k = 0; k < 3; ++k) f.wf[k] = tr8(wp, k * 8192);
}

DI void mma(f32x16 (&acc)[4], const Frags& f) {
#pragma unroll
  for (int k = 0; k < 3; ++k)
#pragma unroll
    for (int t = 0; t < 4; ++t) {
      int s = t - k + 1;
      if (s >= 0 && s < 4) acc[s] = __builtin_amdgcn_mfma_f32_32x32x16_bf16(f.wf[k], f.xf[t], acc[s], 0, 0, 0);
    }
}

DI float bf_lo(unsigned u) { return __builtin_bit_cast(float, u << 16); }
DI float bf_hi(unsigned u) { return __builtin_bit_cast(float, u & 0xffff0000u); }
DI unsigned pack2(float a, float b) {
  f32x2 v = {a, b};
  return __builtin_bit_cast(unsigned, __builtin_convertvector(v, bf16x2));
}

__global__ __launch_bounds__(512, 2) void conv_bn_silu_kernel(
    const __hip_bfloat16* __restrict__ x, const __hip_bfloat16* __restrict__ w,
    const __hip_bfloat16* __restrict__ mean, const __hip_bfloat16* __restrict__ var,
    const __hip_bfloat16* __restrict__ gam, const __hip_bfloat16* __restrict__ bet,
    __hip_bfloat16* __restrict__ out STAMP_ARG) {
  STAMP_DECL
  __shared__ __attribute__((aligned(1024))) char smem[4 * STAGE_B];
  const unsigned tid = threadIdx.x;
  const unsigned wave = rfl(tid >> 6);
  const unsigned lane = tid & 63;
  const unsigned bid = blockIdx.x;
  const unsigned xcd = bid & 7, idx = bid >> 3;
  const unsigned mb = 8 * (xcd >> 1) + (idx & 7);
  const unsigned nb = 4 * (xcd & 1) + (idx >> 3);
  const unsigned lds0 = (unsigned)(size_t)(LAS char*)smem;

  if (wave >= 4) {
    const unsigned li = wave - 4;
    __amdgpu_buffer_rsrc_t sx = __builtin_amdgcn_make_buffer_rsrc((void*)x, (short)0, 1024 * 4 * C_ * 2, SRD3);
    __amdgpu_buffer_rsrc_t sw = __builtin_amdgcn_make_buffer_rsrc((void*)w, (short)0, 3 * C_ * C_ * 2, SRD3);
    const unsigned r8 = lane >> 3, p = lane & 7;
    const unsigned vx0 = r8 * 4096 + ((p ^ (lane >> 4)) << 4);
    const unsigned vx1 = r8 * 4096 + ((p ^ (4 | (lane >> 4))) << 4);
    const unsigned vw = r8 * 1024 + ((p ^ (((lane >> 4) & 1) << 2)) << 4);
    const unsigned sx0 = (mb * 32 * 4 + li) * 1024;
    const unsigned mx0 = lds0 + li * 4096;
    auto issue = [&](int s) {
      const unsigned slot = (s & 3) * STAGE_B;
#pragma unroll
      for (int j = 0; j < 4; ++j)
        dma16(mx0 + slot + j * 1024, (j & 1) ? vx1 : vx0, sx, sx0 + j * 32768 + s * 128);
#pragma unroll
      for (int j = 0; j < 6; ++j) {
        const unsigned jj = li * 6 + j;
        dma16(lds0 + slot + W_OFF + jj * 1024, vw, sw,
              (jj >> 3) * (C_ * C_ * 2) + (s * 64 + (jj & 7) * 8) * 1024 + nb * 128);
      }
    };
#pragma unroll
    for (int s = 0; s < 4; ++s) issue(s);
    STAMP(2);
    wait_vm<30>(); STAMP(3); bar();
    wait_vm<20>(); STAMP(4); bar(); issue(4);
    wait_vm<20>(); STAMP(5); bar(); issue(5);
    wait_vm<20>(); STAMP(6); bar(); issue(6);
    wait_vm<20>(); STAMP(7); bar(); issue(7);
    wait_vm<20>(); STAMP(8); bar();
    wait_vm<10>(); STAMP(9); bar();
    wait_vm<0>(); STAMP(10); bar();
    bar();
    STAMP_END;
    return;
  }

  const unsigned kh = wave >> 1, ni = wave & 1;
  const unsigned r = lane & 31, h = lane >> 5;
  const unsigned d0 = nb * 64 + ni * 32 + 8 * h;

  u32x4 pm[2], pv[2], pg[2], pb[2];
#pragma unroll
  for (int i = 0; i < 2; ++i) {
    pm[i] = *(const u32x4*)(mean + d0 + 16 * i);
    pv[i] = *(const u32x4*)(var + d0 + 16 * i);
    pg[i] = *(const u32x4*)(gam + d0 + 16 * i);
    pb[i] = *(const u32x4*)(bet + d0 + 16 * i);
  }
  float sc[16], sh[16];
#pragma unroll
  for (int i = 0; i < 16; ++i) {
    unsigned um = pm[i >> 3][(i >> 1) & 3], uv = pv[i >> 3][(i >> 1) & 3];
    unsigned ug = pg[i >> 3][(i >> 1) & 3], ub = pb[i >> 3][(i >> 1) & 3];
    float m = (i & 1) ? bf_hi(um) : bf_lo(um), v = (i & 1) ? bf_hi(uv) : bf_lo(uv);
    float g = (i & 1) ? bf_hi(ug) : bf_lo(ug), b = (i & 1) ? bf_hi(ub) : bf_lo(ub);
    sc[i] = g * __builtin_amdgcn_rsqf(v + 1e-5f);
    sh[i] = b - m * sc[i];
  }

  const unsigned q = (lane & 15) >> 2, p = lane & 3, G = lane >> 4;
  const unsigned xo0 = r * 128 + (((kh * 4 + h) ^ ((r >> 1) & 7)) << 4);
  const unsigned xo1 = xo0 ^ 32;
  const unsigned wo = W_OFF + (kh * 32 + 8 * h + q) * 128 +
                      (((ni * 4 + 2 * (G & 1) + (p & 1)) ^ ((q >> 1) << 2)) << 4) + 8 * (p >> 1);
  const LAS char* sb = (const LAS char*)smem;

  f32x16 acc[4];
#pragma unroll
  for (int s = 0; s < 4; ++s)
#pragma unroll
    for (int i = 0; i < 16; ++i) acc[s][i] = 0.f;

  Frags fa, fb;
  STAMP(2);
  bar();
  STAMP(3);
  read_frags(fa, sb + xo0, sb + wo);
#pragma unroll
  for (int s = 0; s < NSTAGE; ++s) {
    const int slot = (s & 3) * STAGE_B;
    read_frags(fb, sb + slot + xo1, sb + slot + wo + 2048);
    mma(acc, fa);
    if (s + 1 < NSTAGE) {
      const int nslot = ((s + 1) & 3) * STAGE_B;
      bar_lgkm();
      STAMP(4 + s);
      read_frags(fa, sb + nslot + xo0, sb + nslot + wo);
    }
    mma(acc, fb);
  }

  LAS char* ex = (LAS char*)smem;
  {
    LAS char* dst = ex + wave * 8192 + lane * 16;
#pragma unroll
    for (int a = 0; a < 2; ++a)
#pragma unroll
      for (int i = 0; i < 4; ++i) {
        f32x4 v0 = {acc[a][4 * i], acc[a][4 * i + 1], acc[a][4 * i + 2], acc[a][4 * i + 3]};
        f32x4 v1 = {acc[2 + a][4 * i], acc[2 + a][4 * i + 1], acc[2 + a][4 * i + 2], acc[2 + a][4 * i + 3]};
        f32x4 v = kh ? v0 : v1;
        *(LAS f32x4*)(dst + (a * 4 + i) * 1024) = v;
      }
  }
  STAMP(11);
  bar_lgkm();
  STAMP(12);
  const LAS char* src = ex + (wave ^ 2) * 8192 + lane * 16;
#pragma unroll
  for (int a = 0; a < 2; ++a) {
    float y[16];
#pragma unroll
    for (int i = 0; i < 4; ++i) {
      f32x4 v = *(const LAS f32x4*)(src + (a * 4 + i) * 1024);
#pragma unroll
      for (int j = 0; j < 4; ++j) {
        float own = kh ? acc[2 + a][4 * i + j] : acc[a][4 * i + j];
        y[4 * i + j] = own + v[j];
      }
    }
    unsigned pk[8];
#pragma unroll
    for (int i = 0; i < 16; i += 2) {
      float y0 = y[i] * sc[i] + sh[i], y1 = y[i + 1] * sc[i + 1] + sh[i + 1];
      float e0 = __builtin_amdgcn_exp2f(y0 * -1.4426950408889634f);
      float e1 = __builtin_amdgcn_exp2f(y1 * -1.4426950408889634f);
      float o0 = y0 * __builtin_amdgcn_rcpf(1.f + e0), o1 = y1 * __builtin_amdgcn_rcpf(1.f + e1);
      pk[i >> 1] = pack2(o0, o1);
    }
    const unsigned s = kh * 2 + a;
    __hip_bfloat16* op = out + ((mb * 32 + r) * 4 + s) * C_ + d0;
    u32x4 o0 = {pk[0], pk[1], pk[2], pk[3]}, o1 = {pk[4], pk[5], pk[6], pk[7]};
    *(u32x4*)op = o0;
    *(u32x4*)(op + 16) = o1;
  }
  STAMP(13);
  STAMP_END;
}

__global__ void pre_kernel(unsigned long long* __restrict__ s) {
  if (threadIdx.x == 0) s[256 * 8 * NSTAMP] = __builtin_amdgcn_s_memrealtime();
}
__global__ void report_kernel(const unsigned long long* __restrict__ s) {
  const unsigned long long tstart = __builtin_amdgcn_s_memrealtime();
  unsigned long long rt0 = ~0ull, rt1 = 0, rs1 = 0;
  long long sum = 0;
  for (int b = threadIdx.x; b < 256; b += 64) {
    for (int w = 0; w < 8; ++w) {
      const unsigned long long* p = s + (b * 8 + w) * NSTAMP;
      rt0 = p[0] < rt0 ? p[0] : rt0; rs1 = p[0] > rs1 ? p[0] : rs1; rt1 = p[NSTAMP - 1] > rt1 ? p[NSTAMP - 1] : rt1;
    }
    const unsigned long long* p = s + (b * 8 + 0) * NSTAMP;
    const unsigned long long* q = s + (b * 8 + 4) * NSTAMP;
    sum += (long long)(0);
  }
  for (int o = 32; o; o >>= 1) {
    unsigned long long a = __shfl_xor(rt0, o), b = __shfl_xor(rt1, o), c = __shfl_xor(rs1, o);
    rt0 = a < rt0 ? a : rt0; rt1 = b > rt1 ? b : rt1; rs1 = c > rs1 ? c : rs1;
    sum += __shfl_xor(sum, o);
  }
  long long avg = sum / 256;
  long long span = (long long)(rt1 - rt0), skew = (long long)(rs1 - rt0);
  long long gap = (long long)(tstart - rt1);
  long long pre = (long long)(rt0 - s[256 * 8 * NSTAMP]);
  unsigned long long t0 = __builtin_amdgcn_s_memrealtime();
  long long rdur = (long long)(t0 - tstart);
  long long v = (rdur) * 20;
  if (v < 0) v = 0;
  if (v > 100000) v = 100000;
  while ((long long)(__builtin_amdgcn_s_memrealtime() - t0) < v) __builtin_amdgcn_s_sleep(1);
}

extern "C" void kernel_launch(void* const* d_in, const int* in_sizes, int n_in, void* d_out, int out_size,
                              void* d_ws, size_t ws_size, hipStream_t stream) {
  conv_bn_silu_kernel<<<256, 512, 0, stream>>>(
      (const __hip_bfloat16*)d_in[0], (const __hip_bfloat16*)d_in[1], (const __hip_bfloat16*)d_in[2],
      (const __hip_bfloat16*)d_in[3], (const __hip_bfloat16*)d_in[4], (const __hip_bfloat16*)d_in[5],
      (__hip_bfloat16*)d_out STAMP_PASS);
  report_kernel<<<1, 64, 0, stream>>>((const unsigned long long*)d_ws);
}
```

```cpp
#include <hip/hip_runtime.h>
#include <hip/hip_bf16.h>
#include <stdint.h>

typedef __attribute__((ext_vector_type(8))) __bf16 bf16x8;
typedef __attribute__((ext_vector_type(4))) __bf16 bf16x4;
typedef __attribute__((ext_vector_type(2))) __bf16 bf16x2;
typedef __attribute__((ext_vector_type(4))) short s16x4;
typedef __attribute__((ext_vector_type(16))) float f32x16;
typedef __attribute__((ext_vector_type(4))) float f32x4;
typedef __attribute__((ext_vector_type(2))) float f32x2;
typedef __attribute__((ext_vector_type(4))) unsigned u32x4;
typedef __attribute__((ext_vector_type(2))) unsigned u32x2;

#define LAS __attribute__((address_space(3)))
#define DI __device__ __forceinline__

constexpr int C_ = 512;
constexpr int STAGE_B = 40960;
constexpr int W_OFF = 16384;
constexpr int NSTAGE = 8;
constexpr unsigned SRD3 = 0x00020000u;

DI unsigned rfl(unsigned v) { return __builtin_amdgcn_readfirstlane(v); }

DI void dma16(unsigned m0v, unsigned voff, __amdgpu_buffer_rsrc_t srd, unsigned soff) {
  asm volatile("s_mov_b32 m0, %0\n\ts_nop 0\n\tbuffer_load_dwordx4 %1, %2, %3 offen lds"
               :: "s"(m0v), "v"(voff), "s"(srd), "s"(soff) : "memory");
}

template <int N> DI void wait_vm() { asm volatile("s_waitcnt vmcnt(%0)" :: "n"(N) : "memory"); }
DI void bar() { asm volatile("s_barrier" ::: "memory"); }
DI void bar_lgkm() { asm volatile("s_waitcnt lgkmcnt(0)\n\ts_barrier" ::: "memory"); }

struct Frags { bf16x8 xf[4]; bf16x8 wf[3]; };

DI bf16x8 tr8(const LAS char* p0, int off) {
  s16x4 lo = __builtin_amdgcn_ds_read_tr16_b64_v4i16((LAS s16x4*)(p0 + off));
  s16x4 hi = __builtin_amdgcn_ds_read_tr16_b64_v4i16((LAS s16x4*)(p0 + off + 512));
  typedef __attribute__((ext_vector_type(8))) short s16x8;
  s16x8 v = __builtin_shufflevector(lo, hi, 0, 1, 2, 3, 4, 5, 6, 7);
  return __builtin_bit_cast(bf16x8, v);
}

DI void read_frags(Frags& f, const LAS char* xp, const LAS char* wp) {
#pragma unroll
  for (int t = 0; t < 4; ++t) f.xf[t] = *(const LAS bf16x8*)(xp + t * 4096);
#pragma unroll
  for (int k = 0; k < 3; ++k) f.wf[k] = tr8(wp, k * 8192);
}

DI void mma(f32x16 (&acc)[4], const Frags& f) {
#pragma unroll
  for (int k = 0; k < 3; ++k)
#pragma unroll
    for (int t = 0; t < 4; ++t) {
      int s = t - k + 1;
      if (s >= 0 && s < 4) acc[s] = __builtin_amdgcn_mfma_f32_32x32x16_bf16(f.wf[k], f.xf[t], acc[s], 0, 0, 0);
    }
}

DI float bf_lo(unsigned u) { return __builtin_bit_cast(float, u << 16); }
DI float bf_hi(unsigned u) { return __builtin_bit_cast(float, u & 0xffff0000u); }
DI unsigned pack2(float a, float b) {
  f32x2 v = {a, b};
  return __builtin_bit_cast(unsigned, __builtin_convertvector(v, bf16x2));
}

__global__ __launch_bounds__(512, 2) void conv_bn_silu_kernel(
    const __hip_bfloat16* __restrict__ x, const __hip_bfloat16* __restrict__ w,
    const __hip_bfloat16* __restrict__ mean, const __hip_bfloat16* __restrict__ var,
    const __hip_bfloat16* __restrict__ gam, const __hip_bfloat16* __restrict__ bet,
    __hip_bfloat16* __restrict__ out) {
  __shared__ __attribute__((aligned(1024))) char smem[4 * STAGE_B];
  const unsigned tid = threadIdx.x;
  const unsigned wave = rfl(tid >> 6);
  const unsigned lane = tid & 63;
  const unsigned bid = blockIdx.x;
  const unsigned xcd = bid & 7, idx = bid >> 3;
  const unsigned mb = 8 * (xcd >> 1) + (idx & 7);
  const unsigned nb = 4 * (xcd & 1) + (idx >> 3);
  const unsigned rot = (mb + 2 * nb) & 7;
  const unsigned lds0 = (unsigned)(size_t)(LAS char*)smem;

  if (wave >= 4) {
    const unsigned li = wave - 4;
    __amdgpu_buffer_rsrc_t sx = __builtin_amdgcn_make_buffer_rsrc((void*)x, (short)0, 1024 * 4 * C_ * 2, SRD3);
    __amdgpu_buffer_rsrc_t sw = __builtin_amdgcn_make_buffer_rsrc((void*)w, (short)0, 3 * C_ * C_ * 2, SRD3);
    const unsigned r8 = lane >> 3, p = lane & 7;
    const unsigned vx0 = r8 * 4096 + ((p ^ (lane >> 4)) << 4);
    const unsigned vx1 = r8 * 4096 + ((p ^ (4 | (lane >> 4))) << 4);
    const unsigned vw = r8 * 1024 + ((p ^ (((lane >> 4) & 1) << 2)) << 4);
    const unsigned sx0 = (mb * 32 * 4 + li) * 1024;
    const unsigned mx0 = lds0 + li * 4096;
    auto issue = [&](int s) {
      const unsigned slot = (s & 3) * STAGE_B;
      const unsigned cs = (s + rot) & 7;
#pragma unroll
      for (int j = 0; j < 4; ++j)
        dma16(mx0 + slot + j * 1024, (j & 1) ? vx1 : vx0, sx, sx0 + j * 32768 + cs * 128);
#pragma unroll
      for (int j = 0; j < 6; ++j) {
        const unsigned jj = li * 6 + j;
        dma16(lds0 + slot + W_OFF + jj * 1024, vw, sw,
              (jj >> 3) * (C_ * C_ * 2) + (cs * 64 + (jj & 7) * 8) * 1024 + nb * 128);
      }
    };
#pragma unroll
    for (int s = 0; s < 4; ++s) issue(s);
    wait_vm<30>(); bar();
    wait_vm<20>(); bar(); issue(4);
    wait_vm<20>(); bar(); issue(5);
    wait_vm<20>(); bar(); issue(6);
    wait_vm<20>(); bar(); issue(7);
    wait_vm<20>(); bar();
    wait_vm<10>(); bar();
    wait_vm<0>(); bar();
    bar();
    return;
  }

  const unsigned kh = wave >> 1, ni = wave & 1;
  const unsigned r = lane & 31, h = lane >> 5;
  const unsigned d0 = nb * 64 + ni * 32 + 8 * h;

  u32x4 pm[2], pv[2], pg[2], pb[2];
#pragma unroll
  for (int i = 0; i < 2; ++i) {
    pm[i] = *(const u32x4*)(mean + d0 + 16 * i);
    pv[i] = *(const u32x4*)(var + d0 + 16 * i);
    pg[i] = *(const u32x4*)(gam + d0 + 16 * i);
    pb[i] = *(const u32x4*)(bet + d0 + 16 * i);
  }
  float sc[16], sh[16];
#pragma unroll
  for (int i = 0; i < 16; ++i) {
    unsigned um = pm[i >> 3][(i >> 1) & 3], uv = pv[i >> 3][(i >> 1) & 3];
    unsigned ug = pg[i >> 3][(i >> 1) & 3], ub = pb[i >> 3][(i >> 1) & 3];
    float m = (i & 1) ? bf_hi(um) : bf_lo(um), v = (i & 1) ? bf_hi(uv) : bf_lo(uv);
    float g = (i & 1) ? bf_hi(ug) : bf_lo(ug), b = (i & 1) ? bf_hi(ub) : bf_lo(ub);
    sc[i] = g * __builtin_amdgcn_rsqf(v + 1e-5f);
    sh[i] = b - m * sc[i];
  }

  const unsigned q = (lane & 15) >> 2, p = lane & 3, G = lane >> 4;
  const unsigned xo0 = r * 128 + (((kh * 4 + h) ^ ((r >> 1) & 7)) << 4);
  const unsigned xo1 = xo0 ^ 32;
  const unsigned wo = W_OFF + (kh * 32 + 8 * h + q) * 128 +
                      (((ni * 4 + 2 * (G & 1) + (p & 1)) ^ ((q >> 1) << 2)) << 4) + 8 * (p >> 1);
  const LAS char* sb = (const LAS char*)smem;

  f32x16 acc[4];
#pragma unroll
  for (int s = 0; s < 4; ++s)
#pragma unroll
    for (int i = 0; i < 16; ++i) acc[s][i] = 0.f;

  Frags fa, fb;
  bar();
  read_frags(fa, sb + xo0, sb + wo);
#pragma unroll
  for (int s = 0; s < NSTAGE; ++s) {
    const int slot = (s & 3) * STAGE_B;
    read_frags(fb, sb + slot + xo1, sb + slot + wo + 2048);
    mma(acc, fa);
    if (s + 1 < NSTAGE) {
      const int nslot = ((s + 1) & 3) * STAGE_B;
      bar_lgkm();
      read_frags(fa, sb + nslot + xo0, sb + nslot + wo);
    }
    mma(acc, fb);
  }

  LAS char* ex = (LAS char*)smem;
  {
    LAS char* dst = ex + wave * 8192 + lane * 16;
#pragma unroll
    for (int a = 0; a < 2; ++a)
#pragma unroll
      for (int i = 0; i < 4; ++i) {
        f32x4 v0 = {acc[a][4 * i], acc[a][4 * i + 1], acc[a][4 * i + 2], acc[a][4 * i + 3]};
        f32x4 v1 = {acc[2 + a][4 * i], acc[2 + a][4 * i + 1], acc[2 + a][4 * i + 2], acc[2 + a][4 * i + 3]};
        f32x4 v = kh ? v0 : v1;
        *(LAS f32x4*)(dst + (a * 4 + i) * 1024) = v;
      }
  }
  bar_lgkm();
  const LAS char* src = ex + (wave ^ 2) * 8192 + lane * 16;
#pragma unroll
  for (int a = 0; a < 2; ++a) {
    float y[16];
#pragma unroll
    for (int i = 0; i < 4; ++i) {
      f32x4 v = *(const LAS f32x4*)(src + (a * 4 + i) * 1024);
#pragma unroll
      for (int j = 0; j < 4; ++j) {
        float own = kh ? acc[2 + a][4 * i + j] : acc[a][4 * i + j];
        y[4 * i + j] = own + v[j];
      }
    }
    unsigned pk[8];
#pragma unroll
    for (int i = 0; i < 16; i += 2) {
      float y0 = y[i] * sc[i] + sh[i], y1 = y[i + 1] * sc[i + 1] + sh[i + 1];
      float e0 = __builtin_amdgcn_exp2f(y0 * -1.4426950408889634f);
      float e1 = __builtin_amdgcn_exp2f(y1 * -1.4426950408889634f);
      float o0 = y0 * __builtin_amdgcn_rcpf(1.f + e0), o1 = y1 * __builtin_amdgcn_rcpf(1.f + e1);
      pk[i >> 1] = pack2(o0, o1);
    }
    const unsigned s = kh * 2 + a;
    __hip_bfloat16* op = out + ((mb * 32 + r) * 4 + s) * C_ + d0;
    u32x4 o0 = {pk[0], pk[1], pk[2], pk[3]}, o1 = {pk[4], pk[5], pk[6], pk[7]};
    asm volatile("global_store_dwordx4 %0, %1, off nt\n\tglobal_store_dwordx4 %0, %2, off offset:32 nt" :: "v"(op), "v"(o0), "v"(o1) : "memory");
  }
}

extern "C" void kernel_launch(void* const* d_in, const int* in_sizes, int n_in, void* d_out, int out_size,
                              void* d_ws, size_t ws_size, hipStream_t stream) {
  conv_bn_silu_kernel<<<256, 512, 0, stream>>>(
      (const __hip_bfloat16*)d_in[0], (const __hip_bfloat16*)d_in[1], (const __hip_bfloat16*)d_in[2],
      (const __hip_bfloat16*)d_in[3], (const __hip_bfloat16*)d_in[4], (const __hip_bfloat16*)d_in[5],
      (__hip_bfloat16*)d_out);
}
```

```cpp
#include <hip/hip_runtime.h>
#include <hip/hip_bf16.h>
#include <stdint.h>
#define STAMP_ARG
#define STAMP_PASS
#define STAMP_DECL
#define STAMP(i)
#define STAMP_END

#define BAR_AT 6
#define MAP 0
#define AUXX ""
#define NT_OUT 0

typedef __attribute__((ext_vector_type(8))) __bf16 bf16x8;
typedef __attribute__((ext_vector_type(2))) __bf16 bf16x2;
typedef __attribute__((ext_vector_type(4))) short s16x4;
typedef __attribute__((ext_vector_type(8))) short s16x8;
typedef __attribute__((ext_vector_type(16))) float f32x16;
typedef __attribute__((ext_vector_type(4))) float f32x4;
typedef __attribute__((ext_vector_type(2))) float f32x2;
typedef __attribute__((ext_vector_type(4))) unsigned u32x4;

#define LAS __attribute__((address_space(3)))
#define DI __device__ __forceinline__
#define PIN() __builtin_amdgcn_sched_barrier(0)

constexpr int C_ = 512;
constexpr int STAGE_B = 40960;
constexpr int W_OFF = 16384;
constexpr int NSTAGE = 8;
constexpr unsigned SRD3 = 0x00020000u;
constexpr float NLOG2E = -1.4426950408889634f;

DI unsigned rfl(unsigned v) { return __builtin_amdgcn_readfirstlane(v); }

DI void dma_x(unsigned m0v, unsigned voff, __amdgpu_buffer_rsrc_t srd, unsigned soff) {
  asm volatile("s_mov_b32 m0, %0\n\ts_nop 0\n\tbuffer_load_dwordx4 %1, %2, %3 offen " AUXX " lds"
               :: "s"(m0v), "v"(voff), "s"(srd), "s"(soff) : "memory");
}
DI void dma_w(unsigned m0v, unsigned voff, __amdgpu_buffer_rsrc_t srd, unsigned soff) {
  asm volatile("s_mov_b32 m0, %0\n\ts_nop 0\n\tbuffer_load_dwordx4 %1, %2, %3 offen lds"
               :: "s"(m0v), "v"(voff), "s"(srd), "s"(soff) : "memory");
}

template <int N> DI void wait_vm() { asm volatile("s_waitcnt vmcnt(%0)" :: "n"(N) : "memory"); }
DI void bar() { asm volatile("s_barrier" ::: "memory"); }
DI void bar_lgkm() { asm volatile("s_waitcnt lgkmcnt(0)\n\ts_barrier" ::: "memory"); }

struct Frags { bf16x8 xf[4]; s16x4 wl[3], wh[3]; };

DI void rd_x(Frags& f, int t, const LAS char* xp) { f.xf[t] = *(const LAS bf16x8*)(xp + t * 4096); }
DI void rd_wl(Frags& f, int k, const LAS char* wp) {
  f.wl[k] = __builtin_amdgcn_ds_read_tr16_b64_v4i16((LAS s16x4*)(wp + k * 8192));
}
DI void rd_wh(Frags& f, int k, const LAS char* wp) {
  f.wh[k] = __builtin_amdgcn_ds_read_tr16_b64_v4i16((LAS s16x4*)(wp + k * 8192 + 512));
}
DI void rd(Frags& f, int r, const LAS char* xp, const LAS char* wp) {
  switch (r) {
    case 0: rd_wl(f, 0, wp); break;
    case 1: rd_wh(f, 0, wp); break;
    case 2: rd_x(f, 0, xp); break;
    case 3: rd_x(f, 1, xp); break;
    case 4: rd_x(f, 2, xp); break;
    case 5: rd_wl(f, 1, wp); break;
    case 6: rd_wh(f, 1, wp); break;
    case 7: rd_x(f, 3, xp); break;
    case 8: rd_wl(f, 2, wp); break;
    default: rd_wh(f, 2, wp); break;
  }
}
DI void mm(f32x16 (&acc)[4], const Frags& f, int k, int t) {
  s16x8 wv = __builtin_shufflevector(f.wl[k], f.wh[k], 0, 1, 2, 3, 4, 5, 6, 7);
  acc[t - k + 1] = __builtin_amdgcn_mfma_f32_32x32x16_bf16(__builtin_bit_cast(bf16x8, wv), f.xf[t], acc[t - k + 1], 0, 0, 0);
}
constexpr int ORD_K[10] = {0, 0, 0, 1, 1, 1, 1, 2, 2, 2};
constexpr int ORD_T[10] = {0, 1, 2, 0, 1, 2, 3, 1, 2, 3};
constexpr int LAST_K[2][10] = {{0, 0, 1, 1, 2, 0, 1, 1, 2, 2}, {0, 1, 1, 2, 2, 0, 0, 1, 1, 2}};
constexpr int LAST_T[2][10] = {{1, 2, 2, 3, 3, 0, 0, 1, 1, 2}, {0, 0, 1, 1, 2, 1, 2, 2, 3, 3}};

DI unsigned pack2(float a, float b) {
  f32x2 v = {a, b};
  return __builtin_bit_cast(unsigned, __builtin_convertvector(v, bf16x2));
}
DI void store16(__hip_bfloat16* p, u32x4 v) {
  if (NT_OUT) __builtin_nontemporal_store(v, (u32x4*)p);
  else *(u32x4*)p = v;
}

template <int KH>
DI void consumer(LAS char* smem, unsigned wave, unsigned lane, unsigned mb, unsigned nb,
                 const __hip_bfloat16* __restrict__ mean, const __hip_bfloat16* __restrict__ var,
                 const __hip_bfloat16* __restrict__ gam, const __hip_bfloat16* __restrict__ bet,
                 __hip_bfloat16* __restrict__ out
                 ) {
  const unsigned ni = wave & 1;
  const unsigned r = lane & 31, h = lane >> 5;

  float sc[16], sh[16];
  {
    const unsigned d = nb * 64 + ni * 32 + r;
    const float m = __bfloat162float(mean[d]), v = __bfloat162float(var[d]);
    const float g = __bfloat162float(gam[d]), b = __bfloat162float(bet[d]);
    const float s1 = g * __builtin_amdgcn_rsqf(v + 1e-5f);
    const float s2 = s1 * NLOG2E, h2 = (b - m * s1) * NLOG2E;
#pragma unroll
    for (int i = 0; i < 16; ++i) {
      const int src = (int)(8 * h + (i & 7) + 16 * (i >> 3)) * 4;
      sc[i] = __builtin_bit_cast(float, __builtin_amdgcn_ds_bpermute(src, __builtin_bit_cast(int, s2)));
      sh[i] = __builtin_bit_cast(float, __builtin_amdgcn_ds_bpermute(src, __builtin_bit_cast(int, h2)));
    }
  }

  const unsigned q = (lane & 15) >> 2, p = lane & 3, G = lane >> 4;
  const unsigned xo0 = r * 128 + (((KH * 4 + h) ^ ((r >> 1) & 7)) << 4);
  const unsigned xo1 = xo0 ^ 32;
  const unsigned wo = W_OFF + (KH * 32 + 8 * h + q) * 128 +
                      (((ni * 4 + 2 * (G & 1) + (p & 1)) ^ ((q >> 1) << 2)) << 4) + 8 * (p >> 1);
  const LAS char* sb = (const LAS char*)smem;

  f32x16 acc[4];
#pragma unroll
  for (int s = 0; s < 4; ++s)
#pragma unroll
    for (int i = 0; i < 16; ++i) acc[s][i] = 0.f;
#pragma unroll
  for (int i = 0; i < 16; ++i) asm volatile("" : "+v"(sc[i]), "+v"(sh[i]));
#pragma unroll
  for (int s = 0; s < 4; ++s) asm volatile("" : "+v"(acc[s]));

  Frags fa, fb;
  STAMP(2);
  bar();
  STAMP(3);
  PIN();
#pragma unroll
  for (int rr = 0; rr < 10; ++rr) rd(fa, rr, sb + xo0, sb + wo);
  PIN();
#pragma unroll
  for (int s = 0; s < NSTAGE; ++s) {
    const int slot = (s & 3) * STAGE_B;
#pragma unroll
    for (int i = 0; i < 10; ++i) {
      mm(acc, fa, ORD_K[i], ORD_T[i]);
      PIN();
      rd(fb, i, sb + slot + xo1, sb + slot + wo + 2048);
      PIN();
    }
    if (s + 1 < NSTAGE) {
      const int nslot = ((s + 1) & 3) * STAGE_B;
      constexpr int NG = 10 - BAR_AT;
#pragma unroll
      for (int i = 0; i < 10; ++i) {
        if (i == BAR_AT) { bar(); PIN(); }
        if (i >= BAR_AT) {
          const int g = i - BAR_AT;
          const int r0 = (g * 10) / NG, r1 = ((g + 1) * 10) / NG;
#pragma unroll
          for (int rr = r0; rr < r1; ++rr) rd(fa, rr, sb + nslot + xo0, sb + nslot + wo);
          PIN();
        }
        mm(acc, fb, ORD_K[i], ORD_T[i]);
        PIN();
      }
      if (s == 0) { STAMP(4); }
      if (s == 1) { STAMP(5); }
      if (s == 2) { STAMP(6); }
      if (s == 6) { STAMP(7); }
    }
  }

  LAS char* ex = smem;
  LAS char* dst = ex + wave * 8192 + lane * 16;
  constexpr int SA = KH ? 0 : 3, SB = KH ? 1 : 2;
#pragma unroll
  for (int i = 0; i < 10; ++i) {
    mm(acc, fb, LAST_K[KH][i], LAST_T[KH][i]);
    PIN();
    if (i >= 5 && i < 9) {
      const int j = i - 5;
      const int sidx = j < 2 ? SA : SB;
#pragma unroll
      for (int e = 0; e < 2; ++e) {
        const int c4 = (j & 1) * 2 + e;
        f32x4 v = {acc[sidx][4 * c4], acc[sidx][4 * c4 + 1], acc[sidx][4 * c4 + 2], acc[sidx][4 * c4 + 3]};
        *(LAS f32x4*)(dst + ((sidx & 1) * 4 + c4) * 1024) = v;
      }
      PIN();
    }
  }
  STAMP(8);
  float z[2][16];
#pragma unroll
  for (int a = 0; a < 2; ++a)
#pragma unroll
    for (int i = 0; i < 16; ++i) z[a][i] = acc[KH * 2 + a][i] * sc[i] + sh[i];
  PIN();
  bar_lgkm();
  STAMP(9);
  PIN();
  const LAS char* src = ex + (wave ^ 2) * 8192 + lane * 16;
  f32x4 pv[2][4];
#pragma unroll
  for (int a = 0; a < 2; ++a)
#pragma unroll
    for (int i = 0; i < 4; ++i) pv[a][i] = *(const LAS f32x4*)(src + (a * 4 + i) * 1024);
  PIN();
#pragma unroll
  for (int a = 0; a < 2; ++a) {
    unsigned pk[8];
#pragma unroll
    for (int i = 0; i < 16; i += 2) {
      const float z0 = pv[a][i >> 2][i & 3] * sc[i] + z[a][i];
      const float z1 = pv[a][i >> 2][(i & 3) + 1] * sc[i + 1] + z[a][i + 1];
      const float e0 = __builtin_amdgcn_exp2f(z0), e1 = __builtin_amdgcn_exp2f(z1);
      const float r0 = __builtin_amdgcn_rcpf(e0 * NLOG2E + NLOG2E), r1 = __builtin_amdgcn_rcpf(e1 * NLOG2E + NLOG2E);
      pk[i >> 1] = pack2(z0 * r0, z1 * r1);
    }
    const unsigned s = KH * 2 + a;
    __hip_bfloat16* op = out + ((mb * 32 + r) * 4 + s) * C_ + nb * 64 + ni * 32 + 8 * h;
    u32x4 o0 = {pk[0], pk[1], pk[2], pk[3]}, o1 = {pk[4], pk[5], pk[6], pk[7]};
    store16(op, o0);
    store16(op + 16, o1);
    if (a == 0) { STAMP(10); }
  }
  STAMP(11);
}

__global__ __launch_bounds__(512, 2) void conv_bn_silu_kernel(
    const __hip_bfloat16* __restrict__ x, const __hip_bfloat16* __restrict__ w,
    const __hip_bfloat16* __restrict__ mean, const __hip_bfloat16* __restrict__ var,
    const __hip_bfloat16* __restrict__ gam, const __hip_bfloat16* __restrict__ bet,
    __hip_bfloat16* __restrict__ out STAMP_ARG) {
  STAMP_DECL
  __shared__ __attribute__((aligned(1024))) char smem[4 * STAGE_B];
  const unsigned tid = threadIdx.x;
  const unsigned wave = rfl(tid >> 6);
  const unsigned lane = tid & 63;
  const unsigned bid = blockIdx.x;
  const unsigned xcd = bid & 7, idx = bid >> 3;
  const unsigned mb = MAP == 0 ? 8 * (xcd >> 1) + (idx & 7) : 4 * xcd + (idx & 3);
  const unsigned nb = MAP == 0 ? 4 * (xcd & 1) + (idx >> 3) : (idx >> 2);

  if (wave >= 4) {
    const unsigned lds0 = (unsigned)(size_t)(LAS char*)smem;
    const unsigned rot = MAP == 0 ? (mb + 2 * nb) & 7 : (nb + 2 * mb + xcd) & 7;
    const unsigned li = wave - 4;
    __amdgpu_buffer_rsrc_t sx = __builtin_amdgcn_make_buffer_rsrc((void*)x, (short)0, 1024 * 4 * C_ * 2, SRD3);
    __amdgpu_buffer_rsrc_t sw = __builtin_amdgcn_make_buffer_rsrc((void*)w, (short)0, 3 * C_ * C_ * 2, SRD3);
    const unsigned r8 = lane >> 3, p = lane & 7;
    const unsigned vx0 = r8 * 4096 + ((p ^ (lane >> 4)) << 4);
    const unsigned vx1 = r8 * 4096 + ((p ^ (4 | (lane >> 4))) << 4);
    const unsigned vw = r8 * 1024 + ((p ^ (((lane >> 4) & 1) << 2)) << 4);
    const unsigned sx0 = (mb * 32 * 4 + li) * 1024;
    const unsigned mx0 = lds0 + li * 4096;
    auto issue = [&](int s) {
      const unsigned slot = (s & 3) * STAGE_B;
      const unsigned cs = (s + rot) & 7;
#pragma unroll
      for (int j = 0; j < 4; ++j)
        dma_x(mx0 + slot + j * 1024, (j & 1) ? vx1 : vx0, sx, sx0 + j * 32768 + cs * 128);
#pragma unroll
      for (int j = 0; j < 6; ++j) {
        const unsigned jj = li * 6 + j;
        dma_w(lds0 + slot + W_OFF + jj * 1024, vw, sw,
              (jj >> 3) * (C_ * C_ * 2) + (cs * 64 + (jj & 7) * 8) * 1024 + nb * 128);
      }
    };
    issue(0);
    STAMP(2);
#pragma unroll
    for (int s = 0; s < NSTAGE; ++s) {
      if (s + 1 < NSTAGE) { issue(s + 1); wait_vm<10>(); } else wait_vm<0>();
      STAMP(3 + s);
      bar();
    }
    bar();
    STAMP_END;
    return;
  }
  if (wave >> 1) consumer<1>((LAS char*)smem, wave, lane, mb, nb, mean, var, gam, bet, out);
  else consumer<0>((LAS char*)smem, wave, lane, mb, nb, mean, var, gam, bet, out);
}

extern "C" void kernel_launch(void* const* d_in, const int* in_sizes, int n_in, void* d_out, int out_size,
                              void* d_ws, size_t ws_size, hipStream_t stream) {
  conv_bn_silu_kernel<<<256, 512, 0, stream>>>(
      (const __hip_bfloat16*)d_in[0], (const __hip_bfloat16*)d_in[1], (const __hip_bfloat16*)d_in[2],
      (const __hip_bfloat16*)d_in[3], (const __hip_bfloat16*)d_in[4], (const __hip_bfloat16*)d_in[5],
      (__hip_bfloat16*)d_out STAMP_PASS);
}
```

```cpp
#include <hip/hip_runtime.h>
#include <hip/hip_bf16.h>
#include <stdint.h>
#define STAMP_ARG
#define STAMP_PASS
#define STAMP_DECL
#define STAMP(i)
#define STAMP_END

#define BAR_AT 6
#define MAP 0
#define AUXX ""
#define NT_OUT 0

typedef __attribute__((ext_vector_type(8))) __bf16 bf16x8;
typedef __attribute__((ext_vector_type(2))) __bf16 bf16x2;
typedef __attribute__((ext_vector_type(4))) short s16x4;
typedef __attribute__((ext_vector_type(8))) short s16x8;
typedef __attribute__((ext_vector_type(16))) float f32x16;
typedef __attribute__((ext_vector_type(4))) float f32x4;
typedef __attribute__((ext_vector_type(2))) float f32x2;
typedef __attribute__((ext_vector_type(4))) unsigned u32x4;

#define LAS __attribute__((address_space(3)))
#define DI __device__ __forceinline__
#define PIN() __builtin_amdgcn_sched_barrier(0)

constexpr int C_ = 512;
constexpr int STAGE_B = 40960;
constexpr int W_OFF = 16384;
constexpr int NSTAGE = 8;
constexpr unsigned SRD3 = 0x00020000u;
constexpr float NLOG2E = -1.4426950408889634f;

DI unsigned rfl(unsigned v) { return __builtin_amdgcn_readfirstlane(v); }

DI void dma_x(unsigned m0v, unsigned voff, __amdgpu_buffer_rsrc_t srd, unsigned soff) {
  asm volatile("s_mov_b32 m0, %0\n\ts_nop 0\n\tbuffer_load_dwordx4 %1, %2, %3 offen " AUXX " lds"
               :: "s"(m0v), "v"(voff), "s"(srd), "s"(soff) : "memory");
}
DI void dma_w(unsigned m0v, unsigned voff, __amdgpu_buffer_rsrc_t srd, unsigned soff) {
  asm volatile("s_mov_b32 m0, %0\n\ts_nop 0\n\tbuffer_load_dwordx4 %1, %2, %3 offen lds"
               :: "s"(m0v), "v"(voff), "s"(srd), "s"(soff) : "memory");
}

template <int N> DI void wait_vm() { asm volatile("s_waitcnt vmcnt(%0)" :: "n"(N) : "memory"); }
DI void bar() { asm volatile("s_barrier" ::: "memory"); }
DI void bar_lgkm() { asm volatile("s_waitcnt lgkmcnt(0)\n\ts_barrier" ::: "memory"); }

struct Frags { bf16x8 xf[4]; s16x4 wl[3], wh[3]; };

DI void rd_x(Frags& f, int t, const LAS char* xp) { f.xf[t] = *(const LAS bf16x8*)(xp + t * 4096); }
DI void rd_wl(Frags& f, int k, const LAS char* wp) {
  f.wl[k] = __builtin_amdgcn_ds_read_tr16_b64_v4i16((LAS s16x4*)(wp + k * 8192));
}
DI void rd_wh(Frags& f, int k, const LAS char* wp) {
  f.wh[k] = __builtin_amdgcn_ds_read_tr16_b64_v4i16((LAS s16x4*)(wp + k * 8192 + 512));
}
DI void rd(Frags& f, int r, const LAS char* xp, const LAS char* wp) {
  switch (r) {
    case 0: rd_wl(f, 0, wp); break;
    case 1: rd_wh(f, 0, wp); break;
    case 2: rd_x(f, 0, xp); break;
    case 3: rd_x(f, 1, xp); break;
    case 4: rd_x(f, 2, xp); break;
    case 5: rd_wl(f, 1, wp); break;
    case 6: rd_wh(f, 1, wp); break;
    case 7: rd_x(f, 3, xp); break;
    case 8: rd_wl(f, 2, wp); break;
    default: rd_wh(f, 2, wp); break;
  }
}
DI void mm(f32x16 (&acc)[4], const Frags& f, int k, int t) {
  s16x8 wv = __builtin_shufflevector(f.wl[k], f.wh[k], 0, 1, 2, 3, 4, 5, 6, 7);
  acc[t - k + 1] = __builtin_amdgcn_mfma_f32_32x32x16_bf16(__builtin_bit_cast(bf16x8, wv), f.xf[t], acc[t - k + 1], 0, 0, 0);
}
constexpr int ORD_K[10] = {0, 0, 0, 1, 1, 1, 1, 2, 2, 2};
constexpr int ORD_T[10] = {0, 1, 2, 0, 1, 2, 3, 1, 2, 3};
constexpr int LAST_K[2][10] = {{0, 0, 1, 1, 2, 0, 1, 1, 2, 2}, {0, 1, 1, 2, 2, 0, 0, 1, 1, 2}};
constexpr int LAST_T[2][10] = {{1, 2, 2, 3, 3, 0, 0, 1, 1, 2}, {0, 0, 1, 1, 2, 1, 2, 2, 3, 3}};

DI unsigned pack2(float a, float b) {
  f32x2 v = {a, b};
  return __builtin_bit_cast(unsigned, __builtin_convertvector(v, bf16x2));
}
DI void store16(__hip_bfloat16* p, u32x4 v) {
  if (NT_OUT) __builtin_nontemporal_store(v, (u32x4*)p);
  else *(u32x4*)p = v;
}

template <int KH>
DI void consumer(LAS char* smem, unsigned wave, unsigned lane, unsigned mb, unsigned nb,
                 const __hip_bfloat16* __restrict__ mean, const __hip_bfloat16* __restrict__ var,
                 const __hip_bfloat16* __restrict__ gam, const __hip_bfloat16* __restrict__ bet,
                 __hip_bfloat16* __restrict__ out
                 ) {
  const unsigned ni = wave & 1;
  const unsigned r = lane & 31, h = lane >> 5;

  float sc[16], sh[16];
  {
    const unsigned d = nb * 64 + ni * 32 + r;
    const float m = __bfloat162float(mean[d]), v = __bfloat162float(var[d]);
    const float g = __bfloat162float(gam[d]), b = __bfloat162float(bet[d]);
    const float s1 = g * __builtin_amdgcn_rsqf(v + 1e-5f);
    const float s2 = s1 * NLOG2E, h2 = (b - m * s1) * NLOG2E;
#pragma unroll
    for (int i = 0; i < 16; ++i) {
      const int src = (int)(8 * h + (i & 7) + 16 * (i >> 3)) * 4;
      sc[i] = __builtin_bit_cast(float, __builtin_amdgcn_ds_bpermute(src, __builtin_bit_cast(int, s2)));
      sh[i] = __builtin_bit_cast(float, __builtin_amdgcn_ds_bpermute(src, __builtin_bit_cast(int, h2)));
    }
  }

  const unsigned q = (lane & 15) >> 2, p = lane & 3, G = lane >> 4;
  const unsigned xo0 = r * 128 + (((KH * 4 + h) ^ ((r >> 1) & 7)) << 4);
  const unsigned xo1 = xo0 ^ 32;
  const unsigned wo = W_OFF + (KH * 32 + 8 * h + q) * 128 +
                      (((ni * 4 + 2 * (G & 1) + (p & 1)) ^ ((q >> 1) << 2)) << 4) + 8 * (p >> 1);
  const LAS char* sb = (const LAS char*)smem;

  f32x16 acc[4];
#pragma unroll
  for (int s = 0; s < 4; ++s)
#pragma unroll
    for (int i = 0; i < 16; ++i) acc[s][i] = 0.f;
#pragma unroll
  for (int i = 0; i < 16; ++i) asm volatile("" : "+v"(sc[i]), "+v"(sh[i]));
#pragma unroll
  for (int s = 0; s < 4; ++s) asm volatile("" : "+v"(acc[s]));

  Frags fa, fb;
  STAMP(2);
  bar();
  STAMP(3);
  PIN();
#pragma unroll
  for (int rr = 0; rr < 10; ++rr) rd(fa, rr, sb + xo0, sb + wo);
  PIN();
#pragma unroll
  for (int s = 0; s < NSTAGE; ++s) {
    const int slot = (s & 3) * STAGE_B;
#pragma unroll
    for (int i = 0; i < 10; ++i) {
      mm(acc, fa, ORD_K[i], ORD_T[i]);
      PIN();
      rd(fb, i, sb + slot + xo1, sb + slot + wo + 2048);
      PIN();
    }
    if (s + 1 < NSTAGE) {
      const int nslot = ((s + 1) & 3) * STAGE_B;
      constexpr int NG = 10 - BAR_AT;
#pragma unroll
      for (int i = 0; i < 10; ++i) {
        if (i == BAR_AT) { bar(); PIN(); }
        if (i >= BAR_AT) {
          const int g = i - BAR_AT;
          const int r0 = (g * 10) / NG, r1 = ((g + 1) * 10) / NG;
#pragma unroll
          for (int rr = r0; rr < r1; ++rr) rd(fa, rr, sb + nslot + xo0, sb + nslot + wo);
          PIN();
        }
        mm(acc, fb, ORD_K[i], ORD_T[i]);
        PIN();
      }
      if (s == 0) { STAMP(4); }
      if (s == 1) { STAMP(5); }
      if (s == 2) { STAMP(6); }
      if (s == 6) { STAMP(7); }
    }
  }

  LAS char* ex = smem;
  LAS char* dst = ex + wave * 8192 + lane * 16;
  constexpr int SA = KH ? 0 : 3, SB = KH ? 1 : 2;
#pragma unroll
  for (int i = 0; i < 10; ++i) {
    mm(acc, fb, LAST_K[KH][i], LAST_T[KH][i]);
    PIN();
    if (i >= 5 && i < 9) {
      const int j = i - 5;
      const int sidx = j < 2 ? SA : SB;
#pragma unroll
      for (int e = 0; e < 2; ++e) {
        const int c4 = (j & 1) * 2 + e;
        f32x4 v = {acc[sidx][4 * c4], acc[sidx][4 * c4 + 1], acc[sidx][4 * c4 + 2], acc[sidx][4 * c4 + 3]};
        *(LAS f32x4*)(dst + ((sidx & 1) * 4 + c4) * 1024) = v;
      }
      PIN();
    }
  }
  STAMP(8);
  float z[2][16];
#pragma unroll
  for (int a = 0; a < 2; ++a)
#pragma unroll
    for (int i = 0; i < 16; ++i) z[a][i] = acc[KH * 2 + a][i] * sc[i] + sh[i];
  PIN();
  bar_lgkm();
  STAMP(9);
  PIN();
  const LAS char* src = ex + (wave ^ 2) * 8192 + lane * 16;
  f32x4 pv[2][4];
#pragma unroll
  for (int a = 0; a < 2; ++a)
#pragma unroll
    for (int i = 0; i < 4; ++i) pv[a][i] = *(const LAS f32x4*)(src + (a * 4 + i) * 1024);
  PIN();
#pragma unroll
  for (int a = 0; a < 2; ++a) {
    unsigned pk[8];
#pragma unroll
    for (int i = 0; i < 16; i += 2) {
      const float z0 = pv[a][i >> 2][i & 3] * sc[i] + z[a][i];
      const float z1 = pv[a][i >> 2][(i & 3) + 1] * sc[i + 1] + z[a][i + 1];
      const float e0 = __builtin_amdgcn_exp2f(z0), e1 = __builtin_amdgcn_exp2f(z1);
      const float r0 = __builtin_amdgcn_rcpf(e0 * NLOG2E + NLOG2E), r1 = __builtin_amdgcn_rcpf(e1 * NLOG2E + NLOG2E);
      pk[i >> 1] = pack2(z0 * r0, z1 * r1);
    }
    const unsigned s = KH * 2 + a;
    __hip_bfloat16* op = out + ((mb * 32 + r) * 4 + s) * C_ + nb * 64 + ni * 32 + 8 * h;
    u32x4 o0 = {pk[0], pk[1], pk[2], pk[3]}, o1 = {pk[4], pk[5], pk[6], pk[7]};
    store16(op, o0);
    store16(op + 16, o1);
    if (a == 0) { STAMP(10); }
  }
  STAMP(11);
}

__global__ __launch_bounds__(512, 2) void conv_bn_silu_kernel(
    const __hip_bfloat16* __restrict__ x, const __hip_bfloat16* __restrict__ w,
    const __hip_bfloat16* __restrict__ mean, const __hip_bfloat16* __restrict__ var,
    const __hip_bfloat16* __restrict__ gam, const __hip_bfloat16* __restrict__ bet,
    __hip_bfloat16* __restrict__ out STAMP_ARG) {
  STAMP_DECL
  __shared__ __attribute__((aligned(1024))) char smem[4 * STAGE_B];
  const unsigned tid = threadIdx.x;
  const unsigned wave = rfl(tid >> 6);
  const unsigned lane = tid & 63;
  const unsigned bid = blockIdx.x;
  const unsigned xcd = bid & 7, idx = bid >> 3;
  const unsigned mb = MAP == 0 ? 8 * (xcd >> 1) + (idx & 7) : 4 * xcd + (idx & 3);
  const unsigned nb = MAP == 0 ? 4 * (xcd & 1) + (idx >> 3) : (idx >> 2);

  if (wave >= 4) {
    const unsigned lds0 = (unsigned)(size_t)(LAS char*)smem;
    const unsigned rot = MAP == 0 ? (mb + 2 * nb) & 7 : (nb + 2 * mb + xcd) & 7;
    const unsigned li = wave - 4;
    __amdgpu_buffer_rsrc_t sx = __builtin_amdgcn_make_buffer_rsrc((void*)x, (short)0, 1024 * 4 * C_ * 2, SRD3);
    __amdgpu_buffer_rsrc_t sw = __builtin_amdgcn_make_buffer_rsrc((void*)w, (short)0, 3 * C_ * C_ * 2, SRD3);
    const unsigned r8 = lane >> 3, p = lane & 7;
    const unsigned vx0 = r8 * 4096 + ((p ^ (lane >> 4)) << 4);
    const unsigned vx1 = r8 * 4096 + ((p ^ (4 | (lane >> 4))) << 4);
    const unsigned vw = r8 * 1024 + ((p ^ (((lane >> 4) & 1) << 2)) << 4);
    const unsigned sx0 = (mb * 32 * 4 + li) * 1024;
    const unsigned mx0 = lds0 + li * 4096;
    auto issue = [&](int s) {
      const unsigned slot = (s & 3) * STAGE_B;
      const unsigned cs = (s + rot) & 7;
#pragma unroll
      for (int j = 0; j < 4; ++j)
        dma_x(mx0 + slot + j * 1024, (j & 1) ? vx1 : vx0, sx, sx0 + j * 32768 + cs * 128);
#pragma unroll
      for (int j = 0; j < 6; ++j) {
        const unsigned jj = li * 6 + j;
        dma_w(lds0 + slot + W_OFF + jj * 1024, vw, sw,
              (jj >> 3) * (C_ * C_ * 2) + (cs * 64 + (jj & 7) * 8) * 1024 + nb * 128);
      }
    };
    issue(0);
    STAMP(2);
#pragma unroll
    for (int s = 0; s < NSTAGE; ++s) {
      if (s + 1 < NSTAGE) { issue(s + 1); wait_vm<10>(); } else wait_vm<0>();
      STAMP(3 + s);
      bar();
    }
    bar();
    STAMP_END;
    return;
  }
  if (wave >> 1) consumer<1>((LAS char*)smem, wave, lane, mb, nb, mean, var, gam, bet, out);
  else consumer<0>((LAS char*)smem, wave, lane, mb, nb, mean, var, gam, bet, out);
}

constexpr int NW = 512 / 64;
__global__ __launch_bounds__(512, 1) void probe_kernel(unsigned long long* __restrict__ s, __hip_bfloat16* __restrict__ out) {
  const unsigned long long t0 = __builtin_amdgcn_s_memrealtime();
  __shared__ char smem[163840 > 0 ? 163840 : 4];
  asm volatile("v_mov_b32 v150, 0" ::: "v150");
  if (163840 > 0) smem[threadIdx.x] = 1;
  __syncthreads();
  unsigned long long t1 = t0;
  while ((long long)(t1 - t0) < 300) { __builtin_amdgcn_s_sleep(2); t1 = __builtin_amdgcn_s_memrealtime(); }
  if ((threadIdx.x & 63) == 0) { s[(blockIdx.x * 8 + (threadIdx.x >> 6)) * 2] = t0; s[(blockIdx.x * 8 + (threadIdx.x >> 6)) * 2 + 1] = t1 + smem[0]; }
}
__global__ void report_kernel(const unsigned long long* __restrict__ s) {
  unsigned long long mn = ~0ull, mx = 0, mx0 = 0; long long sum = 0;
  for (int b = threadIdx.x; b < 256; b += 64) {
    for (int w = 0; w < NW; ++w) { unsigned long long t = s[(b * 8 + w) * 2]; mn = t < mn ? t : mn; mx = t > mx ? t : mx; }
    unsigned long long t = s[(b * 8) * 2]; mx0 = t > mx0 ? t : mx0;
  }
  for (int o = 32; o; o >>= 1) {
    unsigned long long a = __shfl_xor(mn, o), b = __shfl_xor(mx, o), c = __shfl_xor(mx0, o);
    mn = a < mn ? a : mn; mx = b > mx ? b : mx; mx0 = c > mx0 ? c : mx0;
  }
  for (int b = threadIdx.x; b < 256; b += 64) sum += (long long)(s[(b * 8) * 2] - mn);
  for (int o = 32; o; o >>= 1) sum += __shfl_xor(sum, o);
  long long skew = (long long)(mx - mn), skew0 = (long long)(mx0 - mn), avg = sum / 256;
  unsigned long long t0 = __builtin_amdgcn_s_memrealtime();
  long long v = (0) * 10;
  if (v < 0) v = 0;
  if (v > 100000) v = 100000;
  while ((long long)(__builtin_amdgcn_s_memrealtime() - t0) < v) __builtin_amdgcn_s_sleep(1);
}
extern "C" void kernel_launch(void* const* d_in, const int* in_sizes, int n_in, void* d_out, int out_size,
                              void* d_ws, size_t ws_size, hipStream_t stream) {
  probe_kernel<<<256, 512, 0, stream>>>((unsigned long long*)d_ws, (__hip_bfloat16*)d_out);
  conv_bn_silu_kernel<<<256, 512, 0, stream>>>(
      (const __hip_bfloat16*)d_in[0], (const __hip_bfloat16*)d_in[1], (const __hip_bfloat16*)d_in[2],
      (const __hip_bfloat16*)d_in[3], (const __hip_bfloat16*)d_in[4], (const __hip_bfloat16*)d_in[5],
      (__hip_bfloat16*)d_out STAMP_PASS);
  report_kernel<<<1, 64, 0, stream>>>((const unsigned long long*)d_ws);
}
```

```cpp
#include <hip/hip_runtime.h>
#include <hip/hip_bf16.h>
#include <stdint.h>
#define STAMP_ARG
#define STAMP_PASS
#define STAMP_DECL
#define STAMP(i)
#define STAMP_END

#define BAR_AT 6
#define MAP 0
#define AUXX ""
#define NT_OUT 0

typedef __attribute__((ext_vector_type(8))) __bf16 bf16x8;
typedef __attribute__((ext_vector_type(2))) __bf16 bf16x2;
typedef __attribute__((ext_vector_type(4))) short s16x4;
typedef __attribute__((ext_vector_type(8))) short s16x8;
typedef __attribute__((ext_vector_type(16))) float f32x16;
typedef __attribute__((ext_vector_type(4))) float f32x4;
typedef __attribute__((ext_vector_type(2))) float f32x2;
typedef __attribute__((ext_vector_type(4))) unsigned u32x4;

#define LAS __attribute__((address_space(3)))
#define DI __device__ __forceinline__
#define PIN() __builtin_amdgcn_sched_barrier(0)

constexpr int C_ = 512;
constexpr int STAGE_B = 40960;
constexpr int W_OFF = 16384;
constexpr int NSTAGE = 8;
constexpr unsigned SRD3 = 0x00020000u;
constexpr float NLOG2E = -1.4426950408889634f;

DI unsigned rfl(unsigned v) { return __builtin_amdgcn_readfirstlane(v); }

DI void dma_x(unsigned m0v, unsigned voff, __amdgpu_buffer_rsrc_t srd, unsigned soff) {
  asm volatile("s_mov_b32 m0, %0\n\ts_nop 0\n\tbuffer_load_dwordx4 %1, %2, %3 offen " AUXX " lds"
               :: "s"(m0v), "v"(voff), "s"(srd), "s"(soff) : "memory");
}
DI void dma_w(unsigned m0v, unsigned voff, __amdgpu_buffer_rsrc_t srd, unsigned soff) {
  asm volatile("s_mov_b32 m0, %0\n\ts_nop 0\n\tbuffer_load_dwordx4 %1, %2, %3 offen lds"
               :: "s"(m0v), "v"(voff), "s"(srd), "s"(soff) : "memory");
}

template <int N> DI void wait_vm() { asm volatile("s_waitcnt vmcnt(%0)" :: "n"(N) : "memory"); }
DI void bar() { asm volatile("s_barrier" ::: "memory"); }
DI void bar_lgkm() { asm volatile("s_waitcnt lgkmcnt(0)\n\ts_barrier" ::: "memory"); }

struct Frags { bf16x8 xf[4]; s16x4 wl[3], wh[3]; };

DI void rd_x(Frags& f, int t, const LAS char* xp) { f.xf[t] = *(const LAS bf16x8*)(xp + t * 4096); }
DI void rd_wl(Frags& f, int k, const LAS char* wp) {
  f.wl[k] = __builtin_amdgcn_ds_read_tr16_b64_v4i16((LAS s16x4*)(wp + k * 8192));
}
DI void rd_wh(Frags& f, int k, const LAS char* wp) {
  f.wh[k] = __builtin_amdgcn_ds_read_tr16_b64_v4i16((LAS s16x4*)(wp + k * 8192 + 512));
}
DI void rd(Frags& f, int r, const LAS char* xp, const LAS char* wp) {
  switch (r) {
    case 0: rd_wl(f, 0, wp); break;
    case 1: rd_wh(f, 0, wp); break;
    case 2: rd_x(f, 0, xp); break;
    case 3: rd_x(f, 1, xp); break;
    case 4: rd_x(f, 2, xp); break;
    case 5: rd_wl(f, 1, wp); break;
    case 6: rd_wh(f, 1, wp); break;
    case 7: rd_x(f, 3, xp); break;
    case 8: rd_wl(f, 2, wp); break;
    default: rd_wh(f, 2, wp); break;
  }
}
DI void mm(f32x16 (&acc)[4], const Frags& f, int k, int t) {
  s16x8 wv = __builtin_shufflevector(f.wl[k], f.wh[k], 0, 1, 2, 3, 4, 5, 6, 7);
  acc[t - k + 1] = __builtin_amdgcn_mfma_f32_32x32x16_bf16(__builtin_bit_cast(bf16x8, wv), f.xf[t], acc[t - k + 1], 0, 0, 0);
}
constexpr int ORD_K[10] = {0, 0, 0, 1, 1, 1, 1, 2, 2, 2};
constexpr int ORD_T[10] = {0, 1, 2, 0, 1, 2, 3, 1, 2, 3};
constexpr int LAST_K[2][10] = {{0, 0, 1, 1, 2, 0, 1, 1, 2, 2}, {0, 1, 1, 2, 2, 0, 0, 1, 1, 2}};
constexpr int LAST_T[2][10] = {{1, 2, 2, 3, 3, 0, 0, 1, 1, 2}, {0, 0, 1, 1, 2, 1, 2, 2, 3, 3}};

DI unsigned pack2(float a, float b) {
  f32x2 v = {a, b};
  return __builtin_bit_cast(unsigned, __builtin_convertvector(v, bf16x2));
}
DI void store16(__hip_bfloat16* p, u32x4 v) {
  if (NT_OUT) __builtin_nontemporal_store(v, (u32x4*)p);
  else *(u32x4*)p = v;
}

template <int KH>
DI void consumer(LAS char* smem, unsigned wave, unsigned lane, unsigned mb, unsigned nb,
                 const __hip_bfloat16* __restrict__ mean, const __hip_bfloat16* __restrict__ var,
                 const __hip_bfloat16* __restrict__ gam, const __hip_bfloat16* __restrict__ bet,
                 __hip_bfloat16* __restrict__ out
                 ) {
  const unsigned ni = wave & 1;
  const unsigned r = lane & 31, h = lane >> 5;

  float sc[16], sh[16];
  {
    const unsigned d = nb * 64 + ni * 32 + r;
    const float m = __bfloat162float(mean[d]), v = __bfloat162float(var[d]);
    const float g = __bfloat162float(gam[d]), b = __bfloat162float(bet[d]);
    const float s1 = g * __builtin_amdgcn_rsqf(v + 1e-5f);
    const float s2 = s1 * NLOG2E, h2 = (b - m * s1) * NLOG2E;
#pragma unroll
    for (int i = 0; i < 16; ++i) {
      const int src = (int)(8 * h + (i & 7) + 16 * (i >> 3)) * 4;
      sc[i] = __builtin_bit_cast(float, __builtin_amdgcn_ds_bpermute(src, __builtin_bit_cast(int, s2)));
      sh[i] = __builtin_bit_cast(float, __builtin_amdgcn_ds_bpermute(src, __builtin_bit_cast(int, h2)));
    }
  }

  const unsigned q = (lane & 15) >> 2, p = lane & 3, G = lane >> 4;
  const unsigned xo0 = r * 128 + (((KH * 4 + h) ^ ((r >> 1) & 7)) << 4);
  const unsigned xo1 = xo0 ^ 32;
  const unsigned wo = W_OFF + (KH * 32 + 8 * h + q) * 128 +
                      (((ni * 4 + 2 * (G & 1) + (p & 1)) ^ ((q >> 1) << 2)) << 4) + 8 * (p >> 1);
  const LAS char* sb = (const LAS char*)smem;

  f32x16 acc[4];
#pragma unroll
  for (int s = 0; s < 4; ++s)
#pragma unroll
    for (int i = 0; i < 16; ++i) acc[s][i] = 0.f;
#pragma unroll
  for (int i = 0; i < 16; ++i) asm volatile("" : "+v"(sc[i]), "+v"(sh[i]));
#pragma unroll
  for (int s = 0; s < 4; ++s) asm volatile("" : "+v"(acc[s]));

  Frags fa, fb;
  STAMP(2);
  bar();
  STAMP(3);
  PIN();
#pragma unroll
  for (int rr = 0; rr < 10; ++rr) rd(fa, rr, sb + xo0, sb + wo);
  PIN();
#pragma unroll
  for (int s = 0; s < NSTAGE; ++s) {
    const int slot = (s & 3) * STAGE_B;
#pragma unroll
    for (int i = 0; i < 10; ++i) {
      mm(acc, fa, ORD_K[i], ORD_T[i]);
      PIN();
      rd(fb, i, sb + slot + xo1, sb + slot + wo + 2048);
      PIN();
    }
    if (s + 1 < NSTAGE) {
      const int nslot = ((s + 1) & 3) * STAGE_B;
      constexpr int NG = 10 - BAR_AT;
#pragma unroll
      for (int i = 0; i < 10; ++i) {
        if (i == BAR_AT) { bar(); PIN(); }
        if (i >= BAR_AT) {
          const int g = i - BAR_AT;
          const int r0 = (g * 10) / NG, r1 = ((g + 1) * 10) / NG;
#pragma unroll
          for (int rr = r0; rr < r1; ++rr) rd(fa, rr, sb + nslot + xo0, sb + nslot + wo);
          PIN();
        }
        mm(acc, fb, ORD_K[i], ORD_T[i]);
        PIN();
      }
      if (s == 0) { STAMP(4); }
      if (s == 1) { STAMP(5); }
      if (s == 2) { STAMP(6); }
      if (s == 6) { STAMP(7); }
    }
  }

  LAS char* ex = smem;
  LAS char* dst = ex + wave * 8192 + lane * 16;
  constexpr int SA = KH ? 0 : 3, SB = KH ? 1 : 2;
#pragma unroll
  for (int i = 0; i < 10; ++i) {
    mm(acc, fb, LAST_K[KH][i], LAST_T[KH][i]);
    PIN();
    if (i >= 5 && i < 9) {
      const int j = i - 5;
      const int sidx = j < 2 ? SA : SB;
#pragma unroll
      for (int e = 0; e < 2; ++e) {
        const int c4 = (j & 1) * 2 + e;
        f32x4 v = {acc[sidx][4 * c4], acc[sidx][4 * c4 + 1], acc[sidx][4 * c4 + 2], acc[sidx][4 * c4 + 3]};
        *(LAS f32x4*)(dst + ((sidx & 1) * 4 + c4) * 1024) = v;
      }
      PIN();
    }
  }
  STAMP(8);
  float z[2][16];
#pragma unroll
  for (int a = 0; a < 2; ++a)
#pragma unroll
    for (int i = 0; i < 16; ++i) z[a][i] = acc[KH * 2 + a][i] * sc[i] + sh[i];
  PIN();
  bar_lgkm();
  STAMP(9);
  PIN();
  const LAS char* src = ex + (wave ^ 2) * 8192 + lane * 16;
  f32x4 pv[2][4];
#pragma unroll
  for (int a = 0; a < 2; ++a)
#pragma unroll
    for (int i = 0; i < 4; ++i) pv[a][i] = *(const LAS f32x4*)(src + (a * 4 + i) * 1024);
  PIN();
#pragma unroll
  for (int a = 0; a < 2; ++a) {
    unsigned pk[8];
#pragma unroll
    for (int i = 0; i < 16; i += 2) {
      const float z0 = pv[a][i >> 2][i & 3] * sc[i] + z[a][i];
      const float z1 = pv[a][i >> 2][(i & 3) + 1] * sc[i + 1] + z[a][i + 1];
      const float e0 = __builtin_amdgcn_exp2f(z0), e1 = __builtin_amdgcn_exp2f(z1);
      const float r0 = __builtin_amdgcn_rcpf(e0 * NLOG2E + NLOG2E), r1 = __builtin_amdgcn_rcpf(e1 * NLOG2E + NLOG2E);
      pk[i >> 1] = pack2(z0 * r0, z1 * r1);
    }
    const unsigned s = KH * 2 + a;
    __hip_bfloat16* op = out + ((mb * 32 + r) * 4 + s) * C_ + nb * 64 + ni * 32 + 8 * h;
    u32x4 o0 = {pk[0], pk[1], pk[2], pk[3]}, o1 = {pk[4], pk[5], pk[6], pk[7]};
    store16(op, o0);
    store16(op + 16, o1);
    if (a == 0) { STAMP(10); }
  }
  STAMP(11);
}

__global__ __launch_bounds__(512, 2) void conv_bn_silu_kernel(
    const __hip_bfloat16* __restrict__ x, const __hip_bfloat16* __restrict__ w,
    const __hip_bfloat16* __restrict__ mean, const __hip_bfloat16* __restrict__ var,
    const __hip_bfloat16* __restrict__ gam, const __hip_bfloat16* __restrict__ bet,
    __hip_bfloat16* __restrict__ out STAMP_ARG) {
  STAMP_DECL
  __shared__ __attribute__((aligned(1024))) char smem[4 * STAGE_B];
  const unsigned tid = threadIdx.x;
  const unsigned wave = rfl(tid >> 6);
  const unsigned lane = tid & 63;
  const unsigned bid = blockIdx.x;
  const unsigned xcd = bid & 7, idx = bid >> 3;
  const unsigned mb = MAP == 0 ? 8 * (xcd >> 1) + (idx & 7) : 4 * xcd + (idx & 3);
  const unsigned nb = MAP == 0 ? 4 * (xcd & 1) + (idx >> 3) : (idx >> 2);

  if (wave >= 4) {
    const unsigned lds0 = (unsigned)(size_t)(LAS char*)smem;
    const unsigned rot = MAP == 0 ? (mb + 2 * nb) & 7 : (nb + 2 * mb + xcd) & 7;
    const unsigned li = wave - 4;
    __amdgpu_buffer_rsrc_t sx = __builtin_amdgcn_make_buffer_rsrc((void*)x, (short)0, 1024 * 4 * C_ * 2, SRD3);
    __amdgpu_buffer_rsrc_t sw = __builtin_amdgcn_make_buffer_rsrc((void*)w, (short)0, 3 * C_ * C_ * 2, SRD3);
    const unsigned r8 = lane >> 3, p = lane & 7;
    const unsigned vx0 = r8 * 4096 + ((p ^ (lane >> 4)) << 4);
    const unsigned vx1 = r8 * 4096 + ((p ^ (4 | (lane >> 4))) << 4);
    const unsigned vw = r8 * 1024 + ((p ^ (((lane >> 4) & 1) << 2)) << 4);
    const unsigned sx0 = (mb * 32 * 4 + li) * 1024;
    const unsigned mx0 = lds0 + li * 4096;
    auto issue = [&](int s) {
      const unsigned slot = (s & 3) * STAGE_B;
      const unsigned cs = (s + rot) & 7;
#pragma unroll
      for (int j = 0; j < 4; ++j)
        dma_x(mx0 + slot + j * 1024, (j & 1) ? vx1 : vx0, sx, sx0 + j * 32768 + cs * 128);
#pragma unroll
      for (int j = 0; j < 6; ++j) {
        const unsigned jj = li * 6 + j;
        dma_w(lds0 + slot + W_OFF + jj * 1024, vw, sw,
              (jj >> 3) * (C_ * C_ * 2) + (cs * 64 + (jj & 7) * 8) * 1024 + nb * 128);
      }
    };
    issue(0);
    STAMP(2);
#pragma unroll
    for (int s = 0; s < NSTAGE; ++s) {
      if (s + 1 < NSTAGE) { issue(s + 1); wait_vm<10>(); } else wait_vm<0>();
      STAMP(3 + s);
      bar();
    }
    bar();
    STAMP_END;
    return;
  }
  if (wave >> 1) consumer<1>((LAS char*)smem, wave, lane, mb, nb, mean, var, gam, bet, out);
  else consumer<0>((LAS char*)smem, wave, lane, mb, nb, mean, var, gam, bet, out);
}

__global__ void pre_kernel(unsigned long long* __restrict__ s) {
  if (threadIdx.x == 0) s[8192] = __builtin_amdgcn_s_memrealtime();
}
__global__ __launch_bounds__(64, 1) void probe_kernel(unsigned long long* __restrict__ s) {
  const unsigned long long t0 = __builtin_amdgcn_s_memrealtime();
  __shared__ char smem[0 > 0 ? 0 : 4];
  asm volatile("v_mov_b32 v8, 0" ::: "v8");
  if (0 > 0) smem[threadIdx.x] = 1;
  __syncthreads();
  if ((threadIdx.x & 63) == 0) { s[(blockIdx.x * 8 + (threadIdx.x >> 6)) * 2] = t0 + (smem[0] >> 7); }
}
__global__ void report_kernel(const unsigned long long* __restrict__ s) {
  unsigned long long mn = ~0ull;
  for (int b = threadIdx.x; b < 256; b += 64) { unsigned long long t = s[(b * 8) * 2]; mn = t < mn ? t : mn; }
  for (int o = 32; o; o >>= 1) { unsigned long long a = __shfl_xor(mn, o); mn = a < mn ? a : mn; }
  long long pre = (long long)(mn - s[8192]);
  unsigned long long t0 = __builtin_amdgcn_s_memrealtime();
  long long v = pre * 30;
  if (v < 0) v = 0;
  if (v > 100000) v = 100000;
  while ((long long)(__builtin_amdgcn_s_memrealtime() - t0) < v) __builtin_amdgcn_s_sleep(1);
}
extern "C" void kernel_launch(void* const* d_in, const int* in_sizes, int n_in, void* d_out, int out_size,
                              void* d_ws, size_t ws_size, hipStream_t stream) {
  pre_kernel<<<1, 64, 0, stream>>>((unsigned long long*)d_ws);
  probe_kernel<<<256, 64, 0, stream>>>((unsigned long long*)d_ws);
  conv_bn_silu_kernel<<<256, 512, 0, stream>>>(
      (const __hip_bfloat16*)d_in[0], (const __hip_bfloat16*)d_in[1], (const __hip_bfloat16*)d_in[2],
      (const __hip_bfloat16*)d_in[3], (const __hip_bfloat16*)d_in[4], (const __hip_bfloat16*)d_in[5],
      (__hip_bfloat16*)d_out STAMP_PASS);
  report_kernel<<<1, 64, 0, stream>>>((const unsigned long long*)d_ws);
}
```

```cpp
#include <hip/hip_runtime.h>
#include <hip/hip_bf16.h>
#include <stdint.h>
#include <utility>
#define STAMP_ARG
#define STAMP_PASS
#define STAMP_DECL
#define STAMP(i)
#define STAMP_END
#define ROTM 6
#define ST_AUX 16

#define MSTAMP(i)
#define BAR_AT 6
#define MAP 0
#define AUXX ""
#define PRETOUCH 1
#define PRE0 2
#define DEPTH 2

typedef __attribute__((ext_vector_type(8))) __bf16 bf16x8;
typedef __attribute__((ext_vector_type(2))) __bf16 bf16x2;
typedef __attribute__((ext_vector_type(4))) short s16x4;
typedef __attribute__((ext_vector_type(8))) short s16x8;
typedef __attribute__((ext_vector_type(16))) float f32x16;
typedef __attribute__((ext_vector_type(4))) float f32x4;
typedef __attribute__((ext_vector_type(2))) float f32x2;
typedef __attribute__((ext_vector_type(4))) unsigned u32x4;

#define LAS __attribute__((address_space(3)))
#define DI __device__ __forceinline__
#define PIN() __builtin_amdgcn_sched_barrier(0)

constexpr int C_ = 512;
constexpr int STAGE_B = 40960;
constexpr int W_OFF = 16384;
constexpr int NSTAGE = 8;
constexpr unsigned SRD3 = 0x00020000u;
constexpr float NLOG2E = -1.4426950408889634f;

DI unsigned rfl(unsigned v) { return __builtin_amdgcn_readfirstlane(v); }

DI void dma_x(unsigned m0v, unsigned voff, __amdgpu_buffer_rsrc_t srd, unsigned soff) {
  asm volatile("s_mov_b32 m0, %0\n\ts_nop 0\n\tbuffer_load_dwordx4 %1, %2, %3 offen " AUXX " lds"
               :: "s"(m0v), "v"(voff), "s"(srd), "s"(soff) : "memory");
}
DI void dma_w(unsigned m0v, unsigned voff, __amdgpu_buffer_rsrc_t srd, unsigned soff) {
  asm volatile("s_mov_b32 m0, %0\n\ts_nop 0\n\tbuffer_load_dwordx4 %1, %2, %3 offen lds"
               :: "s"(m0v), "v"(voff), "s"(srd), "s"(soff) : "memory");
}

template <int N> DI void wait_vm() { asm volatile("s_waitcnt vmcnt(%0)" :: "n"(N) : "memory"); }
DI void bar() { asm volatile("s_barrier" ::: "memory"); }
DI void bar_lgkm() { asm volatile("s_waitcnt lgkmcnt(0)\n\ts_barrier" ::: "memory"); }

struct Frags { bf16x8 xf[3]; s16x4 wl[3], wh[3]; };

DI void rd_x(Frags& f, int t, const LAS char* xp) { f.xf[t] = *(const LAS bf16x8*)(xp + t * 4096); }
DI void rd_wl(Frags& f, int k, const LAS char* wp) {
  f.wl[k] = __builtin_amdgcn_ds_read_tr16_b64_v4i16((LAS s16x4*)(wp + k * 8192));
}
DI void rd_wh(Frags& f, int k, const LAS char* wp) {
  f.wh[k] = __builtin_amdgcn_ds_read_tr16_b64_v4i16((LAS s16x4*)(wp + k * 8192 + 512));
}
DI void rd(Frags& f, int c, const LAS char* xp, const LAS char* wp) {
  if (c < 3) rd_x(f, c, xp); else if (c < 6) rd_wl(f, c - 3, wp); else rd_wh(f, c - 6, wp);
}
DI void mm(f32x16& acc, const Frags& f, int k, int t) {
  s16x8 wv = __builtin_shufflevector(f.wl[k], f.wh[k], 0, 1, 2, 3, 4, 5, 6, 7);
  acc = __builtin_amdgcn_mfma_f32_32x32x16_bf16(f.xf[t], __builtin_bit_cast(bf16x8, wv), acc, 0, 0, 0);
}
constexpr int M_A[5] = {0, 1, 0, 1, 0};
constexpr int M_K[2][5] = {{0, 1, 1, 2, 2}, {0, 0, 1, 1, 2}};
constexpr int M_T[2][5] = {{0, 0, 1, 1, 2}, {0, 1, 1, 2, 2}};
constexpr int R_ORD[2][9] = {{3, 6, 0, 4, 7, 1, 5, 8, 2}, {3, 6, 0, 1, 4, 7, 2, 5, 8}};
constexpr int RA_ORD[2][6] = {{4, 7, 0, 5, 8, 1}, {3, 6, 1, 4, 7, 2}};
constexpr int RB_ORD[2][3] = {{3, 6, 2}, {0, 5, 8}};

DI unsigned pack2(float a, float b) {
  f32x2 v = {a, b};
  return __builtin_bit_cast(unsigned, __builtin_convertvector(v, bf16x2));
}
#define ST_MOD " sc1"
template <int OFF>
DI void store2(__amdgpu_buffer_rsrc_t so, unsigned pk, unsigned voff, unsigned s0, unsigned s1) {
  asm volatile("buffer_store_short %0, %1, %2, %3 offen offset:%5" ST_MOD "\n\t"
               "buffer_store_short_d16_hi %0, %1, %2, %4 offen offset:%5" ST_MOD
               :: "v"(pk), "v"(voff), "s"(so), "s"(s0), "s"(s1), "n"(OFF) : "memory");
}

template <int OFF>
DI void store1(__amdgpu_buffer_rsrc_t so, unsigned pk, unsigned voff, unsigned s0) {
  asm volatile("buffer_store_dword %0, %1, %2, %3 offen offset:%4" ST_MOD
               :: "v"(pk), "v"(voff), "s"(so), "s"(s0), "n"(OFF) : "memory");
}
#define DWORD 1
constexpr unsigned char TAIL[188] = {0x00, 0x01, 0x10, 0x20, 0x30, 0x02, 0x11, 0x40, 0x21, 0x03, 0x31, 0x50, 0x60, 0x04, 0x12, 0x41, 0x70, 0x05, 0x22, 0x32, 0x80, 0x06, 0x51, 0x61, 0x90, 0xa0, 0x07, 0xb0, 0x13, 0x08, 0x42, 0x71, 0x23, 0x09, 0x33, 0x81, 0x52, 0x0a, 0x62, 0x91, 0xa1, 0x0b, 0xb1, 0x14, 0x43, 0x72, 0x24, 0x34, 0x82, 0x53, 0x63, 0x92, 0xa2, 0xb2, 0x15, 0x44, 0x73, 0x25, 0x35, 0x83, 0x54, 0x64, 0x93, 0xa3, 0xb3, 0x16, 0x45, 0x74, 0x26, 0x36, 0x84, 0x55, 0x65, 0x94, 0xa4, 0xb4, 0x17, 0x46, 0x75, 0x27, 0x37, 0x85, 0x56, 0x66, 0x95, 0xa5, 0xb5, 0x18, 0x47, 0x76, 0x28, 0x38, 0x86, 0x57, 0x67, 0x96, 0xa6, 0xb6, 0x19, 0x48, 0x77, 0x29, 0x39, 0x87, 0x58, 0x68, 0x97, 0xa7, 0xb7, 0x1a, 0x49, 0x78, 0x2a, 0x3a, 0x88, 0x59, 0x69, 0x98, 0xa8, 0xb8, 0x1b, 0x4a, 0x79, 0x2b, 0x3b, 0x89, 0x5a, 0x6a, 0x99, 0xa9, 0xb9, 0x1c, 0x4b, 0x7a, 0x2c, 0x3c, 0x8a, 0x5b, 0x6b, 0x9a, 0xaa, 0xba, 0x1d, 0x4c, 0x7b, 0x2d, 0x3d, 0x8b, 0x5c, 0x6c, 0x9b, 0xab, 0xbb, 0x1e, 0x4d, 0x7c, 0x2e, 0x3e, 0x8c, 0x5d, 0x6d, 0x9c, 0xac, 0xbc, 0x1f, 0x4e, 0x7d, 0x2f, 0x3f, 0x8d, 0x5e, 0x6e, 0x9d, 0xad, 0xbd, 0x4f, 0x7e, 0x8e, 0x5f, 0x6f, 0x9e, 0xae, 0xbe, 0x7f, 0x8f, 0x9f, 0xaf, 0xbf};
struct Pair { f32x2 z, d; float t0, t1; unsigned pk, nb; };

constexpr int newer_than(int q) {
  int cnt = 0; bool seen = false;
  auto iss = [&](int st) { if (seen) cnt += 10; if (st == q) seen = true; };
  for (int j = 0; j < PRE0; ++j) iss(j);
  for (int s = 0; s < q; ++s) {
    const int nx = s + DEPTH;
    if (nx < NSTAGE && nx >= PRE0) iss(nx);
  }
  return cnt;
}

#define LASTM 1
#define RB0 0
struct Epi { f32x2 z, t; };
DI void epi1(Epi& e, float a0, float a1, float sc, float sh) {
  const f32x2 a = {a0, a1}, s2 = {sc, sc}, h2 = {sh, sh};
  e.z = __builtin_elementwise_fma(a, s2, h2);
  e.t = f32x2{__builtin_amdgcn_exp2f(e.z[0]), __builtin_amdgcn_exp2f(e.z[1])};
}
DI void epi2(Epi& e, float nl) {
  const f32x2 n2 = {nl, nl};
  const f32x2 d = __builtin_elementwise_fma(e.t, n2, n2);
  e.t = f32x2{__builtin_amdgcn_rcpf(d[0]), __builtin_amdgcn_rcpf(d[1])};
}
template <int OFF>
DI void epi3(const Epi& e, __amdgpu_buffer_rsrc_t so, unsigned op, unsigned s0, unsigned s1) {
  const f32x2 o = e.z * e.t;
  store2<OFF>(so, pack2(o[0], o[1]), op, s0, s1);
}
DI void keep(Epi& e) { asm volatile("" : "+v"(e.z), "+v"(e.t)); }

struct Tail { float sc, sh, nl; unsigned op, sel; __amdgpu_buffer_rsrc_t so; };
#define KEEP(x) asm volatile("" : "+v"(x))
template <int PH, int N>
DI void tail_op(f32x16 (&acc)[2], const Frags (&g)[4], Pair (&e)[16], const Tail& t, const unsigned (&so_row)[16]) {
  constexpr int c = TAIL[N] >> 4, j = TAIL[N] & 15;
  constexpr int a = j < 8 ? 1 : 0, jj = j & 7;
  constexpr int OFF = (a == 1) == (PH == 1) ? 1024 : 0;
  Pair& p = e[j];
  if constexpr (c == 0) mm(acc[0], g[j / 3], M_K[PH][2 * (j % 3)], M_T[PH][2 * (j % 3)]);
  else if constexpr (c == 1) {
    const f32x2 v = {acc[a][2 * jj], acc[a][2 * jj + 1]}, s2 = {t.sc, t.sc}, h2 = {t.sh, t.sh};
    p.z = __builtin_elementwise_fma(v, s2, h2);
    KEEP(p.z);
  } else if constexpr (c == 2) { p.t0 = __builtin_amdgcn_exp2f(p.z[0]); KEEP(p.t0); }
  else if constexpr (c == 3) { p.t1 = __builtin_amdgcn_exp2f(p.z[1]); KEEP(p.t1); }
  else if constexpr (c == 4) {
    const f32x2 u = {p.t0, p.t1}, n2 = {t.nl, t.nl};
    p.d = __builtin_elementwise_fma(u, n2, n2);
    KEEP(p.d);
  } else if constexpr (c == 5) { p.t0 = __builtin_amdgcn_rcpf(p.d[0]); KEEP(p.t0); }
  else if constexpr (c == 6) { p.t1 = __builtin_amdgcn_rcpf(p.d[1]); KEEP(p.t1); }
  else if constexpr (c == 7) {
    const f32x2 u = {p.t0, p.t1};
    p.d = p.z * u;
    KEEP(p.d);
  } else if constexpr (c == 8) { p.pk = pack2(p.d[0], p.d[1]); KEEP(p.pk); }
  else if constexpr (c == 9) { p.nb = __builtin_amdgcn_mov_dpp(p.pk, 0xB1, 0xf, 0xf, true); KEEP(p.nb); }
  else if constexpr (c == 10) { p.pk = __builtin_amdgcn_perm(p.nb, p.pk, t.sel); KEEP(p.pk); }
  else if constexpr (DWORD) store1<OFF>(t.so, p.pk, t.op, so_row[2 * jj]);
  else store2<OFF>(t.so, p.pk, t.op, so_row[2 * jj], so_row[2 * jj + 1]);
  PIN();
}
template <int PH, int... N>
DI void tail_all(f32x16 (&acc)[2], const Frags (&g)[4], Pair (&e)[16], const Tail& t, const unsigned (&so_row)[16],
                 std::integer_sequence<int, N...>) {
  (tail_op<PH, N>(acc, g, e, t, so_row), ...);
}

template <int PH>
DI void consumer(LAS char* smem, unsigned wave, unsigned lane, unsigned mb, unsigned nb,
                 unsigned pm, unsigned pv, unsigned pg, unsigned pb,
                 __hip_bfloat16* __restrict__ out
                 ) {
  const unsigned ni = wave & 1;
  const unsigned r = lane & 31, h = lane >> 5;
  const __amdgpu_buffer_rsrc_t so = __builtin_amdgcn_make_buffer_rsrc((void*)out, (short)0, 1024 * 4 * C_ * 2, SRD3);

  float sc, sh;
  {
    float m, v, g, b;
    asm volatile("s_waitcnt vmcnt(0)\n\tv_lshlrev_b32 %0, 16, %4\n\tv_lshlrev_b32 %1, 16, %5\n\t"
                 "v_lshlrev_b32 %2, 16, %6\n\tv_lshlrev_b32 %3, 16, %7"
                 : "=&v"(m), "=&v"(v), "=&v"(g), "=&v"(b) : "v"(pm), "v"(pv), "v"(pg), "v"(pb));
    const float s1 = g * __builtin_amdgcn_rsqf(v + 1e-5f);
    sc = s1 * NLOG2E;
    sh = (b - m * s1) * NLOG2E;
    asm volatile("" : "+v"(sc), "+v"(sh));
  }
  float nl = NLOG2E;
  asm volatile("" : "+v"(nl));
  unsigned so_row[16];
#pragma unroll
  for (int i = 0; i < 16; ++i) { so_row[i] = ((i & 3) + 8 * (i >> 2)) * 4096; asm volatile("" : "+s"(so_row[i])); }

  const unsigned q = (lane & 15) >> 2, p = lane & 3, G = lane >> 4;
  unsigned xo[4];
#pragma unroll
  for (int kk = 0; kk < 4; ++kk) xo[kk] = PH * 4096 + r * 128 + (((kk * 2 + h) ^ ((r >> 1) & 7)) << 4);
  const unsigned wo = W_OFF + (8 * h + q) * 128 +
                      (((ni * 4 + 2 * (G & 1) + (p >> 1)) ^ ((q >> 1) << 2)) << 4) + 8 * (p & 1);
  const LAS char* sb = (const LAS char*)smem;
  unsigned op = DWORD ? ((mb * 32 + 4 * h + (r & 1)) * 4 + PH * 2) * (C_ * 2) + (nb * 64 + ni * 32 + (r & ~1u)) * 2
                      : ((mb * 32 + 4 * h) * 4 + PH * 2) * (C_ * 2) + (nb * 64 + ni * 32 + r) * 2;
  unsigned sel = (r & 1) ? 0x03020706u : 0x05040100u;
  asm volatile("" : "+v"(op), "+v"(sel));
  constexpr int OFF3 = PH ? 0 : 1024, OFF2 = PH ? 1024 : 0;

  f32x16 acc[2];
#pragma unroll
  for (int s = 0; s < 2; ++s)
#pragma unroll
    for (int i = 0; i < 16; ++i) acc[s][i] = 0.f;
#pragma unroll
  for (int s = 0; s < 2; ++s) asm volatile("" : "+v"(acc[s]));

  Frags f[2];
  STAMP(2);
  bar();
  STAMP(3);
  PIN();
#pragma unroll
  for (int rr = 0; rr < 9; ++rr) rd(f[0], R_ORD[PH][rr], sb + xo[0], sb + wo);
  PIN();
  constexpr int NFULL = LASTM ? NSTAGE - 1 : NSTAGE;
  Frags g[4];
#pragma unroll
  for (int s = 0; s < NFULL; ++s) {
    const int slot = (s & 3) * STAGE_B, nslot = ((s + 1) & 3) * STAGE_B;
#pragma unroll
    for (int kk = 0; kk < 4; ++kk) {
      const bool last = kk == 3;
      const bool more = !(last && s + 1 == NSTAGE);
      const bool tail = LASTM && last && s + 2 == NSTAGE;
#pragma unroll
      for (int i = 0; i < 5; ++i) {
        if (last && more && i == RB0) { bar(); PIN(); }
        mm(acc[M_A[i]], f[kk & 1], M_K[PH][i], M_T[PH][i]);
        PIN();
        if (tail) {
          if (i >= RB0) {
            constexpr int NG = 5 - RB0, NR = 14;
            const int r0 = ((i - RB0) * NR) / NG, r1 = ((i - RB0 + 1) * NR) / NG;
#pragma unroll
            for (int rr = r0; rr < r1; ++rr)
              rd(g[rr / 6], RA_ORD[PH][rr % 6], sb + nslot + xo[rr / 6], sb + nslot + wo + (rr / 6) * 2048);
          }
        } else if (more) {
          const int i0 = last ? RB0 : 0;
          if (i >= i0) {
            const int NG = 5 - i0;
            const int r0 = ((i - i0) * 9) / NG, r1 = ((i - i0 + 1) * 9) / NG;
#pragma unroll
            for (int rr = r0; rr < r1; ++rr) {
              if (last) rd(f[0], R_ORD[PH][rr], sb + nslot + xo[0], sb + nslot + wo);
              else rd(f[(kk + 1) & 1], R_ORD[PH][rr], sb + slot + xo[kk + 1], sb + slot + wo + (kk + 1) * 2048);
            }
          }
        }
        PIN();
      }
    }
    if (s == 0) { STAMP(4); }
    if (s == 1) { STAMP(5); }
    if (s == 2) { STAMP(6); }
    if (s == 5) { STAMP(7); }
  }

  {
    constexpr int slot = ((NSTAGE - 1) & 3) * STAGE_B;
#pragma unroll
    for (int i = 0; i < 8; ++i) {
      const int kk = i >> 1;
      mm(acc[1], g[kk], M_K[PH][1 + 2 * (i & 1)], M_T[PH][1 + 2 * (i & 1)]);
      PIN();
      constexpr int NR = 10 + 12;
      const int r0 = (i * NR) / 7, r1 = i < 7 ? ((i + 1) * NR) / 7 : r0;
#pragma unroll
      for (int rr = r0; rr < r1; ++rr) {
        if (rr < 10) { const int a = rr + 14; rd(g[a / 6], RA_ORD[PH][a % 6], sb + slot + xo[a / 6], sb + slot + wo + (a / 6) * 2048); }
        else { const int a = rr - 10; rd(g[a / 3], RB_ORD[PH][a % 3], sb + slot + xo[a / 3], sb + slot + wo + (a / 3) * 2048); }
      }
      PIN();
    }
    MSTAMP(8);
    Tail t{sc, sh, nl, op, sel, so};
    Pair e[16];
    tail_all<PH>(acc, g, e, t, so_row, std::make_integer_sequence<int, (int)sizeof(TAIL)>{});
    STAMP(11);
  }
}

__global__ __launch_bounds__(512, 2) void conv_bn_silu_kernel(
    const __hip_bfloat16* __restrict__ x, const __hip_bfloat16* __restrict__ w,
    const __hip_bfloat16* __restrict__ mean, const __hip_bfloat16* __restrict__ var,
    const __hip_bfloat16* __restrict__ gam, const __hip_bfloat16* __restrict__ bet,
    __hip_bfloat16* __restrict__ out STAMP_ARG) {
  STAMP_DECL
  __shared__ __attribute__((aligned(1024))) char smem[4 * STAGE_B];
  const unsigned tid = threadIdx.x;
  const unsigned wave = rfl(tid >> 6);
  const unsigned lane = tid & 63;
  const unsigned bid = blockIdx.x;
  const unsigned xcd = bid & 7, idx = bid >> 3;
  const unsigned mb = MAP == 0 ? 8 * (xcd >> 1) + (idx & 7) : 4 * xcd + (idx & 3);
  const unsigned nb = MAP == 0 ? 4 * (xcd & 1) + (idx >> 3) : (idx >> 2);

  unsigned long long pc;
  unsigned code_len;
  asm volatile("s_getpc_b64 %0\nconv_code_begin:\n\ts_mov_b32 %1, conv_code_end-conv_code_begin" : "=s"(pc), "=s"(code_len));
  unsigned pf;
  {
    const unsigned off = min((idx * 8 + (wave & 3) * 2 + (lane & 1)) * 64u, code_len - 4);
    asm volatile("s_cmp_lt_u32 %3, 4\n\ts_cbranch_scc1 1f\n\tglobal_load_dword %0, %1, %2\n1:"
                 : "=v"(pf) : "v"(off), "s"(pc), "s"(wave) : "memory", "scc");
  }
  const unsigned lds0 = (unsigned)(size_t)(LAS char*)smem;
  unsigned rot = ROTM == 0 ? 0 : ROTM == 1 ? (mb + 2 * nb) & 7 : ROTM == 2 ? ((mb + nb) & 1) * 4 : ROTM == 3 ? (((mb & 7) + nb) & 3) * 2 : ROTM == 4 ? (xcd & 1) * 4 : ROTM == 5 ? (((mb & 7) + (nb & 3)) % 3) * 3
    : ROTM == 6 ? ((((mb & 7) + (nb & 3)) % 3) * 3 + (xcd >> 1)) & 7 : ROTM == 7 ? ((((mb & 7) + (nb & 3)) % 3) * 3 + xcd) & 7
    : ROTM == 8 ? ((((mb & 7) + nb) & 3) * 2 + (xcd >> 1)) & 7 : ROTM == 9 ? (((mb + nb) & 1) * 4 + xcd) & 7
    : ROTM == 10 ? xcd : ROTM == 11 ? (xcd >> 1) * 2 : ROTM == 12 ? (xcd + ((mb + nb) & 1)) & 7
    : ROTM == 13 ? (xcd * 3 + ((mb + nb) & 1) * 2) & 7 : ROTM == 14 ? (xcd + (((mb & 7) + (nb & 3)) % 3)) & 7
    : ROTM == 15 ? mb & 7 : ROTM == 16 ? (nb & 3) * 2 : ROTM == 17 ? ((nb & 3) * 2 + (xcd >> 1)) & 7
    : ROTM == 18 ? (mb + xcd) & 7 : ROTM == 19 ? ((mb & 3) * 2) : ROTM == 20 ? ((mb & 3) * 2 + (xcd >> 1)) & 7 : ((mb & 1) * 4 + (xcd >> 1)) & 7;
  const unsigned li = wave & 3;
  const unsigned r8 = lane >> 3, p = lane & 7;
  unsigned vx0 = r8 * 4096 + ((p ^ (lane >> 4)) << 4);
  unsigned vx1 = r8 * 4096 + ((p ^ (4 | (lane >> 4))) << 4);
  unsigned vw = r8 * 1024 + ((p ^ (((lane >> 4) & 1) << 2)) << 4);
  unsigned sx0 = (mb * 32 * 4 + li) * 1024;
  unsigned mx0 = lds0 + li * 4096;
  asm volatile("" : "+s"(rot), "+s"(sx0), "+s"(mx0));
  asm volatile("" : "+v"(vx0), "+v"(vx1), "+v"(vw));
  asm volatile("" :: "s"(x), "s"(w), "s"(mean), "s"(var), "s"(gam), "s"(bet), "s"(out));
  unsigned pm, pv, pg, pb;
  {
    const unsigned off = (nb * 64 + (wave & 1) * 32 + (lane & 31)) * 2;
    asm volatile("s_cmp_ge_u32 %9, 4\n\ts_cbranch_scc1 1f\n\t"
                 "global_load_ushort %0, %4, %5\n\tglobal_load_ushort %1, %4, %6\n\t"
                 "global_load_ushort %2, %4, %7\n\tglobal_load_ushort %3, %4, %8\n1:"
                 : "=&v"(pm), "=&v"(pv), "=&v"(pg), "=&v"(pb)
                 : "v"(off), "s"(mean), "s"(var), "s"(gam), "s"(bet), "s"(wave) : "memory", "scc");
  }
  if (wave < 4) {
  if (wave >> 1) consumer<1>((LAS char*)smem, wave, lane, mb, nb, pm, pv, pg, pb, out);
  else consumer<0>((LAS char*)smem, wave, lane, mb, nb, pm, pv, pg, pb, out);
    asm volatile("conv_code_end:");
    return;
  }
    __amdgpu_buffer_rsrc_t sx = __builtin_amdgcn_make_buffer_rsrc((void*)x, (short)0, 1024 * 4 * C_ * 2, SRD3);
    __amdgpu_buffer_rsrc_t sw = __builtin_amdgcn_make_buffer_rsrc((void*)w, (short)0, 3 * C_ * C_ * 2, SRD3);
    auto issue = [&](int s) {
      const unsigned slot = (s & 3) * STAGE_B;
      const unsigned cs = (s + rot) & 7;
#pragma unroll
      for (int j = 0; j < 4; ++j)
        dma_x(mx0 + slot + j * 1024, (j & 1) ? vx1 : vx0, sx, sx0 + j * 32768 + cs * 128);
#pragma unroll
      for (int j = 0; j < 6; ++j) {
        const unsigned jj = li * 6 + j;
        dma_w(lds0 + slot + W_OFF + jj * 1024, vw, sw,
              (jj >> 3) * (C_ * C_ * 2) + (cs * 64 + (jj & 7) * 8) * 1024 + nb * 128);
      }
    };
    issue(0);
    STAMP(2);
    if (PRE0 >= 2) issue(1);
    if (PRE0 >= 3) issue(2);
    if (PRE0 >= 4) issue(3);
    STAMP(11);
#pragma unroll
    for (int s = 0; s < NSTAGE; ++s) {
      switch (s) {
        case 0: wait_vm<newer_than(0)>(); break;
        case 1: wait_vm<newer_than(1)>(); break;
        case 2: wait_vm<newer_than(2)>(); break;
        case 3: wait_vm<newer_than(3)>(); break;
        case 4: wait_vm<newer_than(4)>(); break;
        case 5: wait_vm<newer_than(5)>(); break;
        case 6: wait_vm<newer_than(6)>(); break;
        default: wait_vm<newer_than(7)>(); break;
      }
      STAMP(3 + s);
      bar();
      const int nx = s + DEPTH;
      if (nx < NSTAGE && nx >= PRE0) issue(nx);
    }
    asm volatile("" :: "v"(pf));
    STAMP_END;
}

extern "C" void kernel_launch(void* const* d_in, const int* in_sizes, int n_in, void* d_out, int out_size,
                              void* d_ws, size_t ws_size, hipStream_t stream) {
  conv_bn_silu_kernel<<<256, 512, 0, stream>>>(
      (const __hip_bfloat16*)d_in[0], (const __hip_bfloat16*)d_in[1], (const __hip_bfloat16*)d_in[2],
      (const __hip_bfloat16*)d_in[3], (const __hip_bfloat16*)d_in[4], (const __hip_bfloat16*)d_in[5],
      (__hip_bfloat16*)d_out STAMP_PASS);
}
```

```cpp
#include <hip/hip_runtime.h>
#include <hip/hip_bf16.h>
#include <stdint.h>
#include <utility>

typedef __attribute__((ext_vector_type(8))) __bf16 bf16x8;
typedef __attribute__((ext_vector_type(2))) __bf16 bf16x2;
typedef __attribute__((ext_vector_type(4))) short s16x4;
typedef __attribute__((ext_vector_type(8))) short s16x8;
typedef __attribute__((ext_vector_type(16))) float f32x16;
typedef __attribute__((ext_vector_type(2))) float f32x2;

#define LAS __attribute__((address_space(3)))
#define DI __device__ __forceinline__
#define PIN() __builtin_amdgcn_sched_barrier(0)
#define KEEP(x) asm volatile("" : "+v"(x))

constexpr int C_ = 512;
constexpr int STAGE_B = 40960;
constexpr int W_OFF = 16384;
constexpr int NSTAGE = 8;
constexpr int DEPTH = 2;
constexpr unsigned SRD3 = 0x00020000u;
constexpr float NLOG2E = -1.4426950408889634f;

DI unsigned rfl(unsigned v) { return __builtin_amdgcn_readfirstlane(v); }

DI void dma(unsigned m0v, unsigned voff, __amdgpu_buffer_rsrc_t srd, unsigned soff) {
  asm volatile("s_mov_b32 m0, %0\n\ts_nop 0\n\tbuffer_load_dwordx4 %1, %2, %3 offen lds"
               :: "s"(m0v), "v"(voff), "s"(srd), "s"(soff) : "memory");
}
template <int N> DI void wait_vm() { asm volatile("s_waitcnt vmcnt(%0)" :: "n"(N) : "memory"); }
DI void bar() { asm volatile("s_barrier" ::: "memory"); }

struct Frags { bf16x8 xf[3]; s16x4 wl[3], wh[3]; };

DI void rd(Frags& f, int c, const LAS char* xp, const LAS char* wp) {
  if (c < 3) f.xf[c] = *(const LAS bf16x8*)(xp + c * 4096);
  else if (c < 6) f.wl[c - 3] = __builtin_amdgcn_ds_read_tr16_b64_v4i16((LAS s16x4*)(wp + (c - 3) * 8192));
  else f.wh[c - 6] = __builtin_amdgcn_ds_read_tr16_b64_v4i16((LAS s16x4*)(wp + (c - 6) * 8192 + 512));
}
DI void mm(f32x16& acc, const Frags& f, int k, int t) {
  const s16x8 wv = __builtin_shufflevector(f.wl[k], f.wh[k], 0, 1, 2, 3, 4, 5, 6, 7);
  acc = __builtin_amdgcn_mfma_f32_32x32x16_bf16(f.xf[t], __builtin_bit_cast(bf16x8, wv), acc, 0, 0, 0);
}
constexpr int M_A[5] = {0, 1, 0, 1, 0};
constexpr int M_K[2][5] = {{0, 1, 1, 2, 2}, {0, 0, 1, 1, 2}};
constexpr int M_T[2][5] = {{0, 0, 1, 1, 2}, {0, 1, 1, 2, 2}};
constexpr int R_ORD[2][9] = {{3, 6, 0, 4, 7, 1, 5, 8, 2}, {3, 6, 0, 1, 4, 7, 2, 5, 8}};
constexpr int RA_ORD[2][6] = {{4, 7, 0, 5, 8, 1}, {3, 6, 1, 4, 7, 2}};
constexpr int RB_ORD[2][3] = {{3, 6, 2}, {0, 5, 8}};

DI unsigned pack2(float a, float b) {
  const f32x2 v = {a, b};
  return __builtin_bit_cast(unsigned, __builtin_convertvector(v, bf16x2));
}
template <int OFF>
DI void store1(__amdgpu_buffer_rsrc_t so, unsigned pk, unsigned voff, unsigned soff) {
  asm volatile("buffer_store_dword %0, %1, %2, %3 offen offset:%4 sc1"
               :: "v"(pk), "v"(voff), "s"(so), "s"(soff), "n"(OFF) : "memory");
}

constexpr unsigned char TAIL[188] = {
    0x00, 0x01, 0x10, 0x20, 0x30, 0x02, 0x11, 0x40, 0x21, 0x03, 0x31, 0x50, 0x60, 0x04, 0x12, 0x41,
    0x70, 0x05, 0x22, 0x32, 0x80, 0x06, 0x51, 0x61, 0x90, 0xa0, 0x07, 0xb0, 0x13, 0x08, 0x42, 0x71,
    0x23, 0x09, 0x33, 0x81, 0x52, 0x0a, 0x62, 0x91, 0xa1, 0x0b, 0xb1, 0x14, 0x43, 0x72, 0x24, 0x34,
    0x82, 0x53, 0x63, 0x92, 0xa2, 0xb2, 0x15, 0x44, 0x73, 0x25, 0x35, 0x83, 0x54, 0x64, 0x93, 0xa3,
    0xb3, 0x16, 0x45, 0x74, 0x26, 0x36, 0x84, 0x55, 0x65, 0x94, 0xa4, 0xb4, 0x17, 0x46, 0x75, 0x27,
    0x37, 0x85, 0x56, 0x66, 0x95, 0xa5, 0xb5, 0x18, 0x47, 0x76, 0x28, 0x38, 0x86, 0x57, 0x67, 0x96,
    0xa6, 0xb6, 0x19, 0x48, 0x77, 0x29, 0x39, 0x87, 0x58, 0x68, 0x97, 0xa7, 0xb7, 0x1a, 0x49, 0x78,
    0x2a, 0x3a, 0x88, 0x59, 0x69, 0x98, 0xa8, 0xb8, 0x1b, 0x4a, 0x79, 0x2b, 0x3b, 0x89, 0x5a, 0x6a,
    0x99, 0xa9, 0xb9, 0x1c, 0x4b, 0x7a, 0x2c, 0x3c, 0x8a, 0x5b, 0x6b, 0x9a, 0xaa, 0xba, 0x1d, 0x4c,
    0x7b, 0x2d, 0x3d, 0x8b, 0x5c, 0x6c, 0x9b, 0xab, 0xbb, 0x1e, 0x4d, 0x7c, 0x2e, 0x3e, 0x8c, 0x5d,
    0x6d, 0x9c, 0xac, 0xbc, 0x1f, 0x4e, 0x7d, 0x2f, 0x3f, 0x8d, 0x5e, 0x6e, 0x9d, 0xad, 0xbd, 0x4f,
    0x7e, 0x8e, 0x5f, 0x6f, 0x9e, 0xae, 0xbe, 0x7f, 0x8f, 0x9f, 0xaf, 0xbf};

struct Pair { f32x2 z, d; float t0, t1; unsigned pk, nb; };
struct Tail { float sc, sh, nl; unsigned op, sel; __amdgpu_buffer_rsrc_t so; };

template <int PH, int N>
DI void tail_op(f32x16 (&acc)[2], const Frags (&g)[4], Pair (&e)[16], const Tail& t, const unsigned (&so_row)[16]) {
  constexpr int c = TAIL[N] >> 4, j = TAIL[N] & 15;
  constexpr int a = j < 8 ? 1 : 0, jj = j & 7;
  constexpr int OFF = (a == 1) == (PH == 1) ? 1024 : 0;
  Pair& p = e[j];
  if constexpr (c == 0) {
    mm(acc[0], g[j / 3], M_K[PH][2 * (j % 3)], M_T[PH][2 * (j % 3)]);
  } else if constexpr (c == 1) {
    const f32x2 v = {acc[a][2 * jj], acc[a][2 * jj + 1]}, s2 = {t.sc, t.sc}, h2 = {t.sh, t.sh};
    p.z = __builtin_elementwise_fma(v, s2, h2);
    KEEP(p.z);
  } else if constexpr (c == 2) {
    p.t0 = __builtin_amdgcn_exp2f(p.z[0]);
    KEEP(p.t0);
  } else if constexpr (c == 3) {
    p.t1 = __builtin_amdgcn_exp2f(p.z[1]);
    KEEP(p.t1);
  } else if constexpr (c == 4) {
    const f32x2 u = {p.t0, p.t1}, n2 = {t.nl, t.nl};
    p.d = __builtin_elementwise_fma(u, n2, n2);
    KEEP(p.d);
  } else if constexpr (c == 5) {
    p.t0 = __builtin_amdgcn_rcpf(p.d[0]);
    KEEP(p.t0);
  } else if constexpr (c == 6) {
    p.t1 = __builtin_amdgcn_rcpf(p.d[1]);
    KEEP(p.t1);
  } else if constexpr (c == 7) {
    const f32x2 u = {p.t0, p.t1};
    p.d = p.z * u;
    KEEP(p.d);
  } else if constexpr (c == 8) {
    p.pk = pack2(p.d[0], p.d[1]);
    KEEP(p.pk);
  } else if constexpr (c == 9) {
    p.nb = __builtin_amdgcn_mov_dpp(p.pk, 0xB1, 0xf, 0xf, true);
    KEEP(p.nb);
  } else if constexpr (c == 10) {
    p.pk = __builtin_amdgcn_perm(p.nb, p.pk, t.sel);
    KEEP(p.pk);
  } else {
    store1<OFF>(t.so, p.pk, t.op, so_row[2 * jj]);
  }
  PIN();
}
template <int PH, int... N>
DI void tail_all(f32x16 (&acc)[2], const Frags (&g)[4], Pair (&e)[16], const Tail& t, const unsigned (&so_row)[16],
                 std::integer_sequence<int, N...>) {
  (tail_op<PH, N>(acc, g, e, t, so_row), ...);
}

constexpr int newer_than(int q) { return 10 * ((q + DEPTH < NSTAGE ? q + DEPTH : NSTAGE) - q - 1); }

template <int PH>
DI void consumer(LAS char* smem, unsigned wave, unsigned lane, unsigned mb, unsigned nb,
                 unsigned pm, unsigned pv, unsigned pg, unsigned pb, __hip_bfloat16* __restrict__ out) {
  const unsigned ni = wave & 1;
  const unsigned r = lane & 31, h = lane >> 5;
  const __amdgpu_buffer_rsrc_t so = __builtin_amdgcn_make_buffer_rsrc((void*)out, (short)0, 1024 * 4 * C_ * 2, SRD3);

  float sc, sh;
  {
    float m, v, g, b;
    asm volatile("s_waitcnt vmcnt(0)\n\tv_lshlrev_b32 %0, 16, %4\n\tv_lshlrev_b32 %1, 16, %5\n\t"
                 "v_lshlrev_b32 %2, 16, %6\n\tv_lshlrev_b32 %3, 16, %7"
                 : "=&v"(m), "=&v"(v), "=&v"(g), "=&v"(b) : "v"(pm), "v"(pv), "v"(pg), "v"(pb));
    const float s1 = g * __builtin_amdgcn_rsqf(v + 1e-5f);
    sc = s1 * NLOG2E;
    sh = (b - m * s1) * NLOG2E;
    asm volatile("" : "+v"(sc), "+v"(sh));
  }
  float nl = NLOG2E;
  KEEP(nl);
  unsigned so_row[16];
#pragma unroll
  for (int i = 0; i < 16; ++i) { so_row[i] = ((i & 3) + 8 * (i >> 2)) * 4096; asm volatile("" : "+s"(so_row[i])); }

  const unsigned q = (lane & 15) >> 2, p = lane & 3, G = lane >> 4;
  unsigned xo[4];
#pragma unroll
  for (int kk = 0; kk < 4; ++kk) xo[kk] = PH * 4096 + r * 128 + (((kk * 2 + h) ^ ((r >> 1) & 7)) << 4);
  const unsigned wo = W_OFF + (8 * h + q) * 128 +
                      (((ni * 4 + 2 * (G & 1) + (p >> 1)) ^ ((q >> 1) << 2)) << 4) + 8 * (p & 1);
  const LAS char* sb = (const LAS char*)smem;
  unsigned op = ((mb * 32 + 4 * h + (r & 1)) * 4 + PH * 2) * (C_ * 2) + (nb * 64 + ni * 32 + (r & ~1u)) * 2;
  unsigned sel = (r & 1) ? 0x03020706u : 0x05040100u;
  asm volatile("" : "+v"(op), "+v"(sel));

  f32x16 acc[2];
#pragma unroll
  for (int s = 0; s < 2; ++s)
#pragma unroll
    for (int i = 0; i < 16; ++i) acc[s][i] = 0.f;
#pragma unroll
  for (int s = 0; s < 2; ++s) KEEP(acc[s]);

  Frags f[2], g[4];
  bar();
  PIN();
#pragma unroll
  for (int rr = 0; rr < 9; ++rr) rd(f[0], R_ORD[PH][rr], sb + xo[0], sb + wo);
  PIN();
#pragma unroll
  for (int s = 0; s < NSTAGE - 1; ++s) {
    const int slot = (s & 3) * STAGE_B, nslot = ((s + 1) & 3) * STAGE_B;
#pragma unroll
    for (int kk = 0; kk < 4; ++kk) {
      const bool last = kk == 3;
      const bool tail = last && s + 2 == NSTAGE;
#pragma unroll
      for (int i = 0; i < 5; ++i) {
        if (last && i == 0) { bar(); PIN(); }
        mm(acc[M_A[i]], f[kk & 1], M_K[PH][i], M_T[PH][i]);
        PIN();
        if (tail) {
#pragma unroll
          for (int rr = (i * 14) / 5; rr < ((i + 1) * 14) / 5; ++rr)
            rd(g[rr / 6], RA_ORD[PH][rr % 6], sb + nslot + xo[rr / 6], sb + nslot + wo + (rr / 6) * 2048);
        } else {
#pragma unroll
          for (int rr = (i * 9) / 5; rr < ((i + 1) * 9) / 5; ++rr) {
            if (last) rd(f[0], R_ORD[PH][rr], sb + nslot + xo[0], sb + nslot + wo);
            else rd(f[(kk + 1) & 1], R_ORD[PH][rr], sb + slot + xo[kk + 1], sb + slot + wo + (kk + 1) * 2048);
          }
        }
        PIN();
      }
    }
  }

  constexpr int slot = ((NSTAGE - 1) & 3) * STAGE_B;
#pragma unroll
  for (int i = 0; i < 8; ++i) {
    mm(acc[1], g[i >> 1], M_K[PH][1 + 2 * (i & 1)], M_T[PH][1 + 2 * (i & 1)]);
    PIN();
    constexpr int NR = 10 + 12;
    const int r0 = (i * NR) / 7, r1 = i < 7 ? ((i + 1) * NR) / 7 : r0;
#pragma unroll
    for (int rr = r0; rr < r1; ++rr) {
      if (rr < 10) {
        const int a = rr + 14;
        rd(g[a / 6], RA_ORD[PH][a % 6], sb + slot + xo[a / 6], sb + slot + wo + (a / 6) * 2048);
      } else {
        const int a = rr - 10;
        rd(g[a / 3], RB_ORD[PH][a % 3], sb + slot + xo[a / 3], sb + slot + wo + (a / 3) * 2048);
      }
    }
    PIN();
  }
  const Tail t{sc, sh, nl, op, sel, so};
  Pair e[16];
  tail_all<PH>(acc, g, e, t, so_row, std::make_integer_sequence<int, (int)sizeof(TAIL)>{});
}

__global__ __launch_bounds__(512, 2) void conv_bn_silu_kernel(
    const __hip_bfloat16* __restrict__ x, const __hip_bfloat16* __restrict__ w,
    const __hip_bfloat16* __restrict__ mean, const __hip_bfloat16* __restrict__ var,
    const __hip_bfloat16* __restrict__ gam, const __hip_bfloat16* __restrict__ bet,
    __hip_bfloat16* __restrict__ out) {
  __shared__ __attribute__((aligned(1024))) char smem[4 * STAGE_B];
  const unsigned tid = threadIdx.x;
  const unsigned wave = rfl(tid >> 6);
  const unsigned lane = tid & 63;
  const unsigned bid = blockIdx.x;
  const unsigned xcd = bid & 7, idx = bid >> 3;
  const unsigned mb = 8 * (xcd >> 1) + (idx & 7);
  const unsigned nb = 4 * (xcd & 1) + (idx >> 3);

  unsigned long long pc;
  unsigned code_len, pf;
  asm volatile("s_getpc_b64 %0\nconv_code_begin:\n\ts_mov_b32 %1, conv_code_end-conv_code_begin" : "=s"(pc), "=s"(code_len));
  {
    const unsigned off = min((idx * 8 + (wave & 3) * 2 + (lane & 1)) * 64u, code_len - 4);
    asm volatile("s_cmp_lt_u32 %3, 4\n\ts_cbranch_scc1 1f\n\tglobal_load_dword %0, %1, %2\n1:"
                 : "=v"(pf) : "v"(off), "s"(pc), "s"(wave) : "memory", "scc");
  }
  const unsigned lds0 = (unsigned)(size_t)(LAS char*)smem;
  unsigned rot = ((((mb & 7) + (nb & 3)) % 3) * 3 + (xcd >> 1)) & 7;
  const unsigned li = wave & 3;
  const unsigned r8 = lane >> 3, p = lane & 7;
  unsigned vx0 = r8 * 4096 + ((p ^ (lane >> 4)) << 4);
  unsigned vx1 = r8 * 4096 + ((p ^ (4 | (lane >> 4))) << 4);
  unsigned vw = r8 * 1024 + ((p ^ (((lane >> 4) & 1) << 2)) << 4);
  unsigned sx0 = (mb * 32 * 4 + li) * 1024;
  unsigned mx0 = lds0 + li * 4096;
  asm volatile("" : "+s"(rot), "+s"(sx0), "+s"(mx0));
  asm volatile("" : "+v"(vx0), "+v"(vx1), "+v"(vw));
  asm volatile("" :: "s"(x), "s"(w), "s"(mean), "s"(var), "s"(gam), "s"(bet), "s"(out));
  unsigned pm, pv, pg, pb;
  {
    const unsigned off = (nb * 64 + (wave & 1) * 32 + (lane & 31)) * 2;
    asm volatile("s_cmp_ge_u32 %9, 4\n\ts_cbranch_scc1 1f\n\t"
                 "global_load_ushort %0, %4, %5\n\tglobal_load_ushort %1, %4, %6\n\t"
                 "global_load_ushort %2, %4, %7\n\tglobal_load_ushort %3, %4, %8\n1:"
                 : "=&v"(pm), "=&v"(pv), "=&v"(pg), "=&v"(pb)
                 : "v"(off), "s"(mean), "s"(var), "s"(gam), "s"(bet), "s"(wave) : "memory", "scc");
  }
  if (wave < 4) {
    if (wave >> 1) consumer<1>((LAS char*)smem, wave, lane, mb, nb, pm, pv, pg, pb, out);
    else consumer<0>((LAS char*)smem, wave, lane, mb, nb, pm, pv, pg, pb, out);
    asm volatile("conv_code_end:");
    return;
  }

  const __amdgpu_buffer_rsrc_t sx = __builtin_amdgcn_make_buffer_rsrc((void*)x, (short)0, 1024 * 4 * C_ * 2, SRD3);
  const __amdgpu_buffer_rsrc_t sw = __builtin_amdgcn_make_buffer_rsrc((void*)w, (short)0, 3 * C_ * C_ * 2, SRD3);
  auto issue = [&](int s) {
    const unsigned sl = (s & 3) * STAGE_B;
    const unsigned cs = (s + rot) & 7;
#pragma unroll
    for (int j = 0; j < 4; ++j)
      dma(mx0 + sl + j * 1024, (j & 1) ? vx1 : vx0, sx, sx0 + j * 32768 + cs * 128);
#pragma unroll
    for (int j = 0; j < 6; ++j) {
      const unsigned jj = li * 6 + j;
      dma(lds0 + sl + W_OFF + jj * 1024, vw, sw,
          (jj >> 3) * (C_ * C_ * 2) + (cs * 64 + (jj & 7) * 8) * 1024 + nb * 128);
    }
  };
  issue(0);
  issue(1);
#pragma unroll
  for (int s = 0; s < NSTAGE; ++s) {
    if (newer_than(s)) wait_vm<newer_than(0)>(); else wait_vm<0>();
    bar();
    if (s + DEPTH < NSTAGE) issue(s + DEPTH);
  }
  asm volatile("" :: "v"(pf));
}

extern "C" void kernel_launch(void* const* d_in, const int* in_sizes, int n_in, void* d_out, int out_size,
                              void* d_ws, size_t ws_size, hipStream_t stream) {
  conv_bn_silu_kernel<<<256, 512, 0, stream>>>(
      (const __hip_bfloat16*)d_in[0], (const __hip_bfloat16*)d_in[1], (const __hip_bfloat16*)d_in[2],
      (const __hip_bfloat16*)d_in[3], (const __hip_bfloat16*)d_in[4], (const __hip_bfloat16*)d_in[5],
      (__hip_bfloat16*)d_out);
}
```

```cpp
#include <hip/hip_runtime.h>
#include <hip/hip_bf16.h>
#include <stdint.h>
#include <utility>

typedef __attribute__((ext_vector_type(8))) __bf16 bf16x8;
typedef __attribute__((ext_vector_type(2))) __bf16 bf16x2;
typedef __attribute__((ext_vector_type(4))) short s16x4;
typedef __attribute__((ext_vector_type(8))) short s16x8;
typedef __attribute__((ext_vector_type(16))) float f32x16;
typedef __attribute__((ext_vector_type(2))) float f32x2;

#define LAS __attribute__((address_space(3)))
#define DI __device__ __forceinline__
#define PIN() __builtin_amdgcn_sched_barrier(0)
#define KEEP(x) asm volatile("" : "+v"(x))

constexpr int C_ = 512;
constexpr int STAGE_B = 40960;
constexpr int W_OFF = 16384;
constexpr int NSTAGE = 8;
constexpr int DEPTH = 2;
constexpr unsigned SRD3 = 0x00020000u;
constexpr float NLOG2E = -1.4426950408889634f;

DI unsigned rfl(unsigned v) { return __builtin_amdgcn_readfirstlane(v); }

DI void dma(unsigned m0v, unsigned voff, __amdgpu_buffer_rsrc_t srd, unsigned soff) {
  asm volatile("s_mov_b32 m0, %0\n\ts_nop 0\n\tbuffer_load_dwordx4 %1, %2, %3 offen lds"
               :: "s"(m0v), "v"(voff), "s"(srd), "s"(soff) : "memory");
}
template <int N> DI void wait_vm() { asm volatile("s_waitcnt vmcnt(%0)" :: "n"(N) : "memory"); }
DI void bar() { asm volatile("s_barrier" ::: "memory"); }

struct Frags { bf16x8 xf[3]; s16x4 wl[3], wh[3]; };

DI void rd(Frags& f, int c, const LAS char* xp, const LAS char* wp) {
  if (c < 3) f.xf[c] = *(const LAS bf16x8*)(xp + c * 4096);
  else if (c < 6) f.wl[c - 3] = __builtin_amdgcn_ds_read_tr16_b64_v4i16((LAS s16x4*)(wp + (c - 3) * 8192));
  else f.wh[c - 6] = __builtin_amdgcn_ds_read_tr16_b64_v4i16((LAS s16x4*)(wp + (c - 6) * 8192 + 512));
}
DI void mm(f32x16& acc, const Frags& f, int k, int t) {
  const s16x8 wv = __builtin_shufflevector(f.wl[k], f.wh[k], 0, 1, 2, 3, 4, 5, 6, 7);
  acc = __builtin_amdgcn_mfma_f32_32x32x16_bf16(f.xf[t], __builtin_bit_cast(bf16x8, wv), acc, 0, 0, 0);
}
constexpr int M_A[5] = {0, 1, 0, 1, 0};
constexpr int M_K[2][5] = {{0, 1, 1, 2, 2}, {0, 0, 1, 1, 2}};
constexpr int M_T[2][5] = {{0, 0, 1, 1, 2}, {0, 1, 1, 2, 2}};
constexpr int R_ORD[2][9] = {{3, 6, 0, 4, 7, 1, 5, 8, 2}, {3, 6, 0, 1, 4, 7, 2, 5, 8}};
constexpr int RA_ORD[2][6] = {{4, 7, 0, 5, 8, 1}, {3, 6, 1, 4, 7, 2}};
constexpr int RB_ORD[2][3] = {{3, 6, 2}, {0, 5, 8}};

DI unsigned pack2(float a, float b) {
  const f32x2 v = {a, b};
  return __builtin_bit_cast(unsigned, __builtin_convertvector(v, bf16x2));
}
template <int OFF>
DI void store1(__amdgpu_buffer_rsrc_t so, unsigned pk, unsigned voff, unsigned soff) {
  asm volatile("buffer_store_dword %0, %1, %2, %3 offen offset:%4 sc1"
               :: "v"(pk), "v"(voff), "s"(so), "s"(soff), "n"(OFF) : "memory");
}

constexpr unsigned char TAIL[188] = {
    0x00, 0x01, 0x10, 0x20, 0x30, 0x02, 0x11, 0x40, 0x21, 0x03, 0x31, 0x50, 0x60, 0x04, 0x12, 0x41,
    0x70, 0x05, 0x22, 0x32, 0x80, 0x06, 0x51, 0x61, 0x90, 0xa0, 0x07, 0xb0, 0x13, 0x08, 0x42, 0x71,
    0x23, 0x09, 0x33, 0x81, 0x52, 0x0a, 0x62, 0x91, 0xa1, 0x0b, 0xb1, 0x14, 0x43, 0x72, 0x24, 0x34,
    0x82, 0x53, 0x63, 0x92, 0xa2, 0xb2, 0x15, 0x44, 0x73, 0x25, 0x35, 0x83, 0x54, 0x64, 0x93, 0xa3,
    0xb3, 0x16, 0x45, 0x74, 0x26, 0x36, 0x84, 0x55, 0x65, 0x94, 0xa4, 0xb4, 0x17, 0x46, 0x75, 0x27,
    0x37, 0x85, 0x56, 0x66, 0x95, 0xa5, 0xb5, 0x18, 0x47, 0x76, 0x28, 0x38, 0x86, 0x57, 0x67, 0x96,
    0xa6, 0xb6, 0x19, 0x48, 0x77, 0x29, 0x39, 0x87, 0x58, 0x68, 0x97, 0xa7, 0xb7, 0x1a, 0x49, 0x78,
    0x2a, 0x3a, 0x88, 0x59, 0x69, 0x98, 0xa8, 0xb8, 0x1b, 0x4a, 0x79, 0x2b, 0x3b, 0x89, 0x5a, 0x6a,
    0x99, 0xa9, 0xb9, 0x1c, 0x4b, 0x7a, 0x2c, 0x3c, 0x8a, 0x5b, 0x6b, 0x9a, 0xaa, 0xba, 0x1d, 0x4c,
    0x7b, 0x2d, 0x3d, 0x8b, 0x5c, 0x6c, 0x9b, 0xab, 0xbb, 0x1e, 0x4d, 0x7c, 0x2e, 0x3e, 0x8c, 0x5d,
    0x6d, 0x9c, 0xac, 0xbc, 0x1f, 0x4e, 0x7d, 0x2f, 0x3f, 0x8d, 0x5e, 0x6e, 0x9d, 0xad, 0xbd, 0x4f,
    0x7e, 0x8e, 0x5f, 0x6f, 0x9e, 0xae, 0xbe, 0x7f, 0x8f, 0x9f, 0xaf, 0xbf};

__device__ void fake_use(...) __asm__("llvm.fake.use");
template <class T> DI void here(const T& x) {
  if constexpr (sizeof(T) == 4) fake_use(__builtin_bit_cast(unsigned, x)); else fake_use(x);
}

struct Pair { f32x2 z, d; float t0, t1; unsigned pk, nb; };
struct Tail { float sc, sh, nl; unsigned op, sel; __amdgpu_buffer_rsrc_t so; };

template <int PH, int N>
DI void tail_op(f32x16 (&acc)[2], const Frags (&g)[4], Pair (&e)[16], const Tail& t, const unsigned (&so_row)[16]) {
  constexpr int c = TAIL[N] >> 4, j = TAIL[N] & 15;
  constexpr int a = j < 8 ? 1 : 0, jj = j & 7;
  constexpr int OFF = (a == 1) == (PH == 1) ? 1024 : 0;
  Pair& p = e[j];
  if constexpr (c == 0) {
    mm(acc[0], g[j / 3], M_K[PH][2 * (j % 3)], M_T[PH][2 * (j % 3)]);
  } else if constexpr (c == 1) {
    const f32x2 v = {acc[a][2 * jj], acc[a][2 * jj + 1]}, s2 = {t.sc, t.sc}, h2 = {t.sh, t.sh};
    p.z = __builtin_elementwise_fma(v, s2, h2);
    here(p.z);
  } else if constexpr (c == 2) {
    p.t0 = __builtin_amdgcn_exp2f(p.z[0]);
    here(p.t0);
  } else if constexpr (c == 3) {
    p.t1 = __builtin_amdgcn_exp2f(p.z[1]);
    here(p.t1);
  } else if constexpr (c == 4) {
    const f32x2 u = {p.t0, p.t1}, n2 = {t.nl, t.nl};
    p.d = __builtin_elementwise_fma(u, n2, n2);
    here(p.d);
  } else if constexpr (c == 5) {
    p.t0 = __builtin_amdgcn_rcpf(p.d[0]);
    here(p.t0);
  } else if constexpr (c == 6) {
    p.t1 = __builtin_amdgcn_rcpf(p.d[1]);
    here(p.t1);
  } else if constexpr (c == 7) {
    const f32x2 u = {p.t0, p.t1};
    p.d = p.z * u;
    here(p.d);
  } else if constexpr (c == 8) {
    p.pk = pack2(p.d[0], p.d[1]);
    here(p.pk);
  } else if constexpr (c == 9) {
    p.nb = __builtin_amdgcn_mov_dpp(p.pk, 0xB1, 0xf, 0xf, true);
    here(p.nb);
  } else if constexpr (c == 10) {
    p.pk = __builtin_amdgcn_perm(p.nb, p.pk, t.sel);
    here(p.pk);
  } else {
    store1<OFF>(t.so, p.pk, t.op, so_row[2 * jj]);
  }
  PIN();
}
template <int PH, int... N>
DI void tail_all(f32x16 (&acc)[2], const Frags (&g)[4], Pair (&e)[16], const Tail& t, const unsigned (&so_row)[16],
                 std::integer_sequence<int, N...>) {
  (tail_op<PH, N>(acc, g, e, t, so_row), ...);
}

constexpr int newer_than(int q) { return 10 * ((q + DEPTH < NSTAGE ? q + DEPTH : NSTAGE) - q - 1); }

template <int PH>
DI void consumer(LAS char* smem, unsigned wave, unsigned lane, unsigned mb, unsigned nb,
                 unsigned pm, unsigned pv, unsigned pg, unsigned pb, __hip_bfloat16* __restrict__ out) {
  const unsigned ni = wave & 1;
  const unsigned r = lane & 31, h = lane >> 5;
  const __amdgpu_buffer_rsrc_t so = __builtin_amdgcn_make_buffer_rsrc((void*)out, (short)0, 1024 * 4 * C_ * 2, SRD3);

  float sc, sh;
  {
    float m, v, g, b;
    asm volatile("s_waitcnt vmcnt(0)\n\tv_lshlrev_b32 %0, 16, %4\n\tv_lshlrev_b32 %1, 16, %5\n\t"
                 "v_lshlrev_b32 %2, 16, %6\n\tv_lshlrev_b32 %3, 16, %7"
                 : "=&v"(m), "=&v"(v), "=&v"(g), "=&v"(b) : "v"(pm), "v"(pv), "v"(pg), "v"(pb));
    const float s1 = g * __builtin_amdgcn_rsqf(v + 1e-5f);
    sc = s1 * NLOG2E;
    sh = (b - m * s1) * NLOG2E;
    asm volatile("" : "+v"(sc), "+v"(sh));
  }
  float nl = NLOG2E;
  KEEP(nl);
  unsigned so_row[16];
#pragma unroll
  for (int i = 0; i < 16; ++i) { so_row[i] = ((i & 3) + 8 * (i >> 2)) * 4096; asm volatile("" : "+s"(so_row[i])); }

  const unsigned q = (lane & 15) >> 2, p = lane & 3, G = lane >> 4;
  unsigned xo[4];
#pragma unroll
  for (int kk = 0; kk < 4; ++kk) xo[kk] = PH * 4096 + r * 128 + (((kk * 2 + h) ^ ((r >> 1) & 7)) << 4);
  const unsigned wo = W_OFF + (8 * h + q) * 128 +
                      (((ni * 4 + 2 * (G & 1) + (p >> 1)) ^ ((q >> 1) << 2)) << 4) + 8 * (p & 1);
  const LAS char* sb = (const LAS char*)smem;
  unsigned op = ((mb * 32 + 4 * h + (r & 1)) * 4 + PH * 2) * (C_ * 2) + (nb * 64 + ni * 32 + (r & ~1u)) * 2;
  unsigned sel = (r & 1) ? 0x03020706u : 0x05040100u;
  asm volatile("" : "+v"(op), "+v"(sel));

  f32x16 acc[2];
#pragma unroll
  for (int s = 0; s < 2; ++s)
#pragma unroll
    for (int i = 0; i < 16; ++i) acc[s][i] = 0.f;
#pragma unroll
  for (int s = 0; s < 2; ++s) KEEP(acc[s]);

  Frags f[2], g[4];
  bar();
  PIN();
#pragma unroll
  for (int rr = 0; rr < 9; ++rr) rd(f[0], R_ORD[PH][rr], sb + xo[0], sb + wo);
  PIN();
#pragma unroll
  for (int s = 0; s < NSTAGE - 1; ++s) {
    const int slot = (s & 3) * STAGE_B, nslot = ((s + 1) & 3) * STAGE_B;
#pragma unroll
    for (int kk = 0; kk < 4; ++kk) {
      const bool last = kk == 3;
      const bool tail = last && s + 2 == NSTAGE;
#pragma unroll
      for (int i = 0; i < 5; ++i) {
        if (last && i == 0) { bar(); PIN(); }
        mm(acc[M_A[i]], f[kk & 1], M_K[PH][i], M_T[PH][i]);
        PIN();
        if (tail) {
#pragma unroll
          for (int rr = (i * 14) / 5; rr < ((i + 1) * 14) / 5; ++rr)
            rd(g[rr / 6], RA_ORD[PH][rr % 6], sb + nslot + xo[rr / 6], sb + nslot + wo + (rr / 6) * 2048);
        } else {
#pragma unroll
          for (int rr = (i * 9) / 5; rr < ((i + 1) * 9) / 5; ++rr) {
            if (last) rd(f[0], R_ORD[PH][rr], sb + nslot + xo[0], sb + nslot + wo);
            else rd(f[(kk + 1) & 1], R_ORD[PH][rr], sb + slot + xo[kk + 1], sb + slot + wo + (kk + 1) * 2048);
          }
        }
        PIN();
      }
    }
  }

  constexpr int slot = ((NSTAGE - 1) & 3) * STAGE_B;
#pragma unroll
  for (int i = 0; i < 8; ++i) {
    mm(acc[1], g[i >> 1], M_K[PH][1 + 2 * (i & 1)], M_T[PH][1 + 2 * (i & 1)]);
    PIN();
    constexpr int NR = 10 + 12;
    const int r0 = (i * NR) / 7, r1 = i < 7 ? ((i + 1) * NR) / 7 : r0;
#pragma unroll
    for (int rr = r0; rr < r1; ++rr) {
      if (rr < 10) {
        const int a = rr + 14;
        rd(g[a / 6], RA_ORD[PH][a % 6], sb + slot + xo[a / 6], sb + slot + wo + (a / 6) * 2048);
      } else {
        const int a = rr - 10;
        rd(g[a / 3], RB_ORD[PH][a % 3], sb + slot + xo[a / 3], sb + slot + wo + (a / 3) * 2048);
      }
    }
    PIN();
  }
  const Tail t{sc, sh, nl, op, sel, so};
  Pair e[16];
  tail_all<PH>(acc, g, e, t, so_row, std::make_integer_sequence<int, (int)sizeof(TAIL)>{});
}

__global__ __launch_bounds__(512, 2) void conv_bn_silu_kernel(
    const __hip_bfloat16* __restrict__ x, const __hip_bfloat16* __restrict__ w,
    const __hip_bfloat16* __restrict__ mean, const __hip_bfloat16* __restrict__ var,
    const __hip_bfloat16* __restrict__ gam, const __hip_bfloat16* __restrict__ bet,
    __hip_bfloat16* __restrict__ out) {
  __shared__ __attribute__((aligned(1024))) char smem[4 * STAGE_B];
  const unsigned tid = threadIdx.x;
  const unsigned wave = rfl(tid >> 6);
  const unsigned lane = tid & 63;
  const unsigned bid = blockIdx.x;
  const unsigned xcd = bid & 7, idx = bid >> 3;
  const unsigned mb = 8 * (xcd >> 1) + (idx & 7);
  const unsigned nb = 4 * (xcd & 1) + (idx >> 3);

  unsigned long long pc;
  unsigned code_len, pf;
  asm volatile("s_getpc_b64 %0\nconv_code_begin:\n\ts_mov_b32 %1, conv_code_end-conv_code_begin" : "=s"(pc), "=s"(code_len));
  {
    const unsigned off = min((idx * 8 + (wave & 3) * 2 + (lane & 1)) * 64u, code_len - 4);
    asm volatile("s_cmp_lt_u32 %3, 4\n\ts_cbranch_scc1 1f\n\tglobal_load_dword %0, %1, %2\n1:"
                 : "=v"(pf) : "v"(off), "s"(pc), "s"(wave) : "memory", "scc");
  }
  const unsigned lds0 = (unsigned)(size_t)(LAS char*)smem;
  unsigned rot = ((((mb & 7) + (nb & 3)) % 3) * 3 + (xcd >> 1)) & 7;
  const unsigned li = wave & 3;
  const unsigned r8 = lane >> 3, p = lane & 7;
  unsigned vx0 = r8 * 4096 + ((p ^ (lane >> 4)) << 4);
  unsigned vx1 = r8 * 4096 + ((p ^ (4 | (lane >> 4))) << 4);
  unsigned vw = r8 * 1024 + ((p ^ (((lane >> 4) & 1) << 2)) << 4);
  unsigned sx0 = (mb * 32 * 4 + li) * 1024;
  unsigned mx0 = lds0 + li * 4096;
  asm volatile("" : "+s"(rot), "+s"(sx0), "+s"(mx0));
  asm volatile("" : "+v"(vx0), "+v"(vx1), "+v"(vw));
  asm volatile("" :: "s"(x), "s"(w), "s"(mean), "s"(var), "s"(gam), "s"(bet), "s"(out));
  unsigned pm, pv, pg, pb;
  {
    const unsigned off = (nb * 64 + (wave & 1) * 32 + (lane & 31)) * 2;
    asm volatile("s_cmp_ge_u32 %9, 4\n\ts_cbranch_scc1 1f\n\t"
                 "global_load_ushort %0, %4, %5\n\tglobal_load_ushort %1, %4, %6\n\t"
                 "global_load_ushort %2, %4, %7\n\tglobal_load_ushort %3, %4, %8\n1:"
                 : "=&v"(pm), "=&v"(pv), "=&v"(pg), "=&v"(pb)
                 : "v"(off), "s"(mean), "s"(var), "s"(gam), "s"(bet), "s"(wave) : "memory", "scc");
  }
  if (wave < 4) {
    if (wave >> 1) consumer<1>((LAS char*)smem, wave, lane, mb, nb, pm, pv, pg, pb, out);
    else consumer<0>((LAS char*)smem, wave, lane, mb, nb, pm, pv, pg, pb, out);
    asm volatile("conv_code_end:");
    return;
  }

  const __amdgpu_buffer_rsrc_t sx = __builtin_amdgcn_make_buffer_rsrc((void*)x, (short)0, 1024 * 4 * C_ * 2, SRD3);
  const __amdgpu_buffer_rsrc_t sw = __builtin_amdgcn_make_buffer_rsrc((void*)w, (short)0, 3 * C_ * C_ * 2, SRD3);
  auto issue = [&](int s) {
    const unsigned sl = (s & 3) * STAGE_B;
    const unsigned cs = (s + rot) & 7;
#pragma unroll
    for (int j = 0; j < 4; ++j)
      dma(mx0 + sl + j * 1024, (j & 1) ? vx1 : vx0, sx, sx0 + j * 32768 + cs * 128);
#pragma unroll
    for (int j = 0; j < 6; ++j) {
      const unsigned jj = li * 6 + j;
      dma(lds0 + sl + W_OFF + jj * 1024, vw, sw,
          (jj >> 3) * (C_ * C_ * 2) + (cs * 64 + (jj & 7) * 8) * 1024 + nb * 128);
    }
  };
  issue(0);
  issue(1);
#pragma unroll
  for (int s = 0; s < NSTAGE; ++s) {
    if (newer_than(s)) wait_vm<newer_than(0)>(); else wait_vm<0>();
    bar();
    if (s + DEPTH < NSTAGE) issue(s + DEPTH);
  }
  asm volatile("" :: "v"(pf));
}

extern "C" void kernel_launch(void* const* d_in, const int* in_sizes, int n_in, void* d_out, int out_size,
                              void* d_ws, size_t ws_size, hipStream_t stream) {
  conv_bn_silu_kernel<<<256, 512, 0, stream>>>(
      (const __hip_bfloat16*)d_in[0], (const __hip_bfloat16*)d_in[1], (const __hip_bfloat16*)d_in[2],
      (const __hip_bfloat16*)d_in[3], (const __hip_bfloat16*)d_in[4], (const __hip_bfloat16*)d_in[5],
      (__hip_bfloat16*)d_out);
}
```
